```python
import torch

def _softmax(x, axis=-1):
    return torch.softmax(x, dim=axis)
def _gelu(x):
    return torch.nn.functional.gelu(x, approximate="tanh")
def _silu(x):
    return torch.nn.functional.silu(x)
def _sigmoid(x):
    return torch.sigmoid(x)
def _layernorm(x, axis=-1, eps=1e-5):
    m = x.mean(dim=axis, keepdim=True)
    v = x.var(dim=axis, keepdim=True, unbiased=False)
    return (x - m) / torch.sqrt(v + eps)
def _scatter_add(tgt, idx, src, axis=0):
    return tgt.clone().index_add_(axis, idx, src)
def _segment_sum(src, seg, n):
    out = torch.zeros((n,) + src.shape[1:], dtype=src.dtype)
    return out.index_add_(0, seg, src)
def _pad(x, pw):
    flat = tuple(v for pair in reversed(pw) for v in pair)
    return torch.nn.functional.pad(x, flat)

kernel_name = 'standalone__dense_activation_relu__prompt'

def setup_inputs():

    _w = (lambda a: (lambda t: t.to(torch.bfloat16) if t.is_floating_point() else t)(torch.as_tensor(a)))
    B, S, D, Dff = 1, 2048, 640, 1536
    return {
        "x": _w((lambda *s: torch.randn(*s, dtype=torch.bfloat16) * 0.05)(B, S, D)),
        "w1": _w((lambda *s: torch.randn(*s, dtype=torch.bfloat16) * 0.05)(D, Dff)),
        "w2": _w((lambda *s: torch.randn(*s, dtype=torch.bfloat16) * 0.05)(Dff, D)),
    }

def reference(x, w1, w2):
    h = torch.einsum("bsd,df->bsf", x, w1)
    zero = torch.zeros_like(h)
    h = torch.maximum(h, zero)
    return torch.einsum("bsf,fd->bsd", h, w2)
```

```cpp
#include <hip/hip_runtime.h>
#include <hip/hip_bf16.h>
#include <stdint.h>

typedef __attribute__((ext_vector_type(8))) short bf16x8;
typedef __attribute__((ext_vector_type(4))) float f32x4;

static constexpr int M = 2048, D = 640, F = 1536;

__device__ __forceinline__ unsigned short f2bf(float x) {
  __hip_bfloat16 b = __float2bfloat16(x);
  return *reinterpret_cast<unsigned short*>(&b);
}

template <int N, int K, bool RELU>
__global__ __launch_bounds__(256) void gemm_simple(const unsigned short* __restrict__ A, const unsigned short* __restrict__ B, unsigned short* __restrict__ C) {
  __shared__ __attribute__((aligned(16))) unsigned short sA[64][40];
  __shared__ __attribute__((aligned(16))) unsigned short sB[64][40];
  const int bm = blockIdx.y * 64, bn = blockIdx.x * 64;
  const int tid = threadIdx.x, lane = tid & 63, w = tid >> 6;
  const int wr = w >> 1, wc = w & 1;
  f32x4 acc[2][2] = {};
  for (int k0 = 0; k0 < K; k0 += 32) {
    {
      int r = tid >> 2, c = (tid & 3) * 8;
      *(uint4*)&sA[r][c] = *(const uint4*)&A[(size_t)(bm + r) * K + k0 + c];
      int kk = tid >> 3, n = (tid & 7) * 8;
      uint4 v = *(const uint4*)&B[(size_t)(k0 + kk) * N + bn + n];
      unsigned short* p = (unsigned short*)&v;
      for (int j = 0; j < 8; ++j) sB[n + j][kk] = p[j];
    }
    __syncthreads();
    bf16x8 a[2], b[2];
    for (int i = 0; i < 2; ++i) {
      a[i] = *(bf16x8*)&sA[wr * 32 + i * 16 + (lane & 15)][(lane >> 4) * 8];
      b[i] = *(bf16x8*)&sB[wc * 32 + i * 16 + (lane & 15)][(lane >> 4) * 8];
    }
    for (int i = 0; i < 2; ++i)
      for (int j = 0; j < 2; ++j)
        acc[i][j] = __builtin_amdgcn_mfma_f32_16x16x32_bf16(a[i], b[j], acc[i][j], 0, 0, 0);
    __syncthreads();
  }
  for (int i = 0; i < 2; ++i)
    for (int j = 0; j < 2; ++j)
      for (int r = 0; r < 4; ++r) {
        float v = acc[i][j][r];
        if (RELU) v = v > 0.f ? v : 0.f;
        C[(size_t)(bm + wr * 32 + i * 16 + (lane >> 4) * 4 + r) * N + bn + wc * 32 + j * 16 + (lane & 15)] = f2bf(v);
      }
}

extern "C" void kernel_launch(void* const* d_in, const int* in_sizes, int n_in,
                              void* d_out, int out_size, void* d_ws, size_t ws_size,
                              hipStream_t stream) {
  const unsigned short* x = (const unsigned short*)d_in[0];
  const unsigned short* w1 = (const unsigned short*)d_in[1];
  const unsigned short* w2 = (const unsigned short*)d_in[2];
  unsigned short* h = (unsigned short*)d_ws;
  unsigned short* out = (unsigned short*)d_out;
  gemm_simple<F, D, true><<<dim3(F / 64, M / 64), 256, 0, stream>>>(x, w1, h);
  gemm_simple<D, F, false><<<dim3(D / 64, M / 64), 256, 0, stream>>>(h, w2, out);
}
```

```cpp
#include <hip/hip_runtime.h>
#include <stdint.h>

__global__ __launch_bounds__(512, 2) void standalone__dense_activation_relu__prompt_kernel(const void* x, const void* w1, const void* w2, void* out, void* ws) {
  __shared__ __attribute__((aligned(16))) char lds[163840];
  asm volatile(
    "s_load_dwordx8 s[64:71], %0, 0x0\n\t"
    "s_load_dwordx2 s[72:73], %0, 0x20\n\t"
    "s_mov_b32 s43, %1\n\t"
    "v_readfirstlane_b32 s40, %2\n\t"
    "v_and_b32 v0, 63, %2\n\t"
    "s_lshr_b32 s40, s40, 6\n\t"
    "s_and_b32 s30, s40, 3\n\t"
    "s_and_b32 s42, s43, 7\n\t"
    "s_lshr_b32 s41, s43, 3\n\t"
    "v_lshlrev_b32 v1, 4, v0\n\t"
    "s_lshr_b32 s80, s42, 1\n\t"
    "s_and_b32 s81, s42, 1\n\t"
    "s_cmp_eq_u32 s80, 3\n\t"
    "s_cselect_b32 s43, 0, s80\n\t"
    "s_lshl_b32 s97, s43, 7\n\t"
    "s_add_u32 s43, s43, 1\n\t"
    "s_cmp_eq_u32 s43, 3\n\t"
    "s_cselect_b32 s43, 0, s43\n\t"
    "s_lshl_b32 s98, s43, 7\n\t"
    "s_add_u32 s43, s43, 1\n\t"
    "s_cmp_eq_u32 s43, 3\n\t"
    "s_cselect_b32 s43, 0, s43\n\t"
    "s_lshl_b32 s99, s43, 7\n\t"
    "s_lshr_b32 s44, s41, 2\n\t"
    "s_lshl_b32 s43, s80, 3\n\t"
    "s_add_u32 s44, s44, s43\n\t"
    "s_and_b32 s45, s41, 3\n\t"
    "s_lshl_b32 s43, s81, 2\n\t"
    "s_add_u32 s45, s45, s43\n\t"
    "s_cmp_ge_u32 s40, 4\n\t"
    "s_cbranch_scc0 L_compute_%=\n\t"
    "v_lshrrev_b32 v48, 3, v0\n\t"
    "v_and_b32 v49, 7, v0\n\t"
    "v_lshrrev_b32 v50, 4, v0\n\t"
    "s_movk_i32 s43, 1280\n\t"
    "v_mul_lo_u32 v52, v48, s43\n\t"
    "v_xor_b32 v53, v49, v50\n\t"
    "v_lshl_add_u32 v2, v53, 4, v52\n\t"
    "v_or_b32 v53, 4, v50\n\t"
    "v_xor_b32 v53, v49, v53\n\t"
    "v_lshl_add_u32 v3, v53, 4, v52\n\t"
    "v_add_u32 v3, 10240, v3\n\t"
    "v_and_b32 v53, 1, v50\n\t"
    "v_lshlrev_b32 v53, 2, v53\n\t"
    "v_xor_b32 v53, v49, v53\n\t"
    "v_lshlrev_b32 v51, 4, v53\n\t"
    "s_movk_i32 s43, 3072\n\t"
    "v_mul_lo_u32 v54, v48, s43\n\t"
    "v_add_u32 v4, v54, v51\n\t"
    "v_add_u32 v5, 23552, v4\n\t"
    "v_add_u32 v6, 47104, v4\n\t"
    "v_add_u32 v7, 70656, v4\n\t"
    "s_lshl_b32 s43, s44, 2\n\t"
    "s_add_u32 s43, s43, s30\n\t"
    "s_mul_i32 s46, s43, 20480\n\t"
    "s_mul_i32 s47, s45, 384\n\t"
    "s_mul_i32 s43, s30, 98304\n\t"
    "s_add_u32 s47, s47, s43\n\t"
    "s_lshl_b32 s52, s30, 12\n\t"
    "s_lshl_b32 s53, s30, 11\n\t"
    "s_lshr_b32 s29, s41, 2\n\t"
    "s_and_b32 s43, s41, 3\n\t"
    "s_add_u32 s29, s29, s43\n\t"
    "s_sub_u32 s43, s29, 5\n\t"
    "s_cmp_ge_u32 s29, 5\n\t"
    "s_cselect_b32 s29, s43, s29\n\t"
    "s_sub_u32 s43, s29, 5\n\t"
    "s_cmp_ge_u32 s29, 5\n\t"
    "s_cselect_b32 s29, s43, s29\n\t"
    "s_add_u32 s43, s29, 0\n\t"
    "s_sub_u32 s44, s43, 5\n\t"
    "s_cmp_ge_u32 s43, 5\n\t"
    "s_cselect_b32 s43, s44, s43\n\t"
    "s_lshl_b32 s44, s43, 8\n\t"
    "s_add_u32 s34, s46, s44\n\t"
    "s_mul_i32 s44, s43, 393216\n\t"
    "s_add_u32 s39, s47, s44\n\t"
    "s_add_u32 s43, s29, 1\n\t"
    "s_sub_u32 s44, s43, 5\n\t"
    "s_cmp_ge_u32 s43, 5\n\t"
    "s_cselect_b32 s43, s44, s43\n\t"
    "s_lshl_b32 s44, s43, 8\n\t"
    "s_add_u32 s35, s46, s44\n\t"
    "s_mul_i32 s44, s43, 393216\n\t"
    "s_add_u32 s76, s47, s44\n\t"
    "s_waitcnt lgkmcnt(0)\n\t"
    "s_mov_b32 s4, s64\n\t"
    "s_and_b32 s5, s65, 0xffff\n\t"
    "s_mov_b32 s6, 2621440\n\t"
    "s_mov_b32 s7, 0x20000\n\t"
    "s_mov_b32 s8, s66\n\t"
    "s_and_b32 s9, s67, 0xffff\n\t"
    "s_mov_b32 s10, 1966080\n\t"
    "s_mov_b32 s11, 0x20000\n\t"
    "s_add_u32 m0, s53, 0\n\t"
    "s_nop 0\n\t"
    "buffer_load_dwordx4 v2, s[4:7], s34 offen offset:0 lds\n\t"
    "s_add_u32 m0, s53, 1024\n\t"
    "s_nop 0\n\t"
    "buffer_load_dwordx4 v3, s[4:7], s34 offen offset:0 lds\n\t"
    "s_add_u32 m0, s53, 8064\n\t"
    "s_nop 0\n\t"
    "buffer_load_dwordx4 v2, s[4:7], s34 offen offset:128 lds\n\t"
    "s_add_u32 m0, s53, 9088\n\t"
    "s_nop 0\n\t"
    "buffer_load_dwordx4 v3, s[4:7], s34 offen offset:128 lds\n\t"
    "s_add_u32 m0, s52, 49152\n\t"
    "s_add_u32 s54, s39, s97\n\t"
    "buffer_load_dwordx4 v4, s[8:11], s54 offen offset:0 lds\n\t"
    "buffer_load_dwordx4 v5, s[8:11], s54 offen offset:1024 lds\n\t"
    "buffer_load_dwordx4 v6, s[8:11], s54 offen offset:2048 lds\n\t"
    "buffer_load_dwordx4 v7, s[8:11], s54 offen offset:3072 lds\n\t"
    "s_mov_b32 s16, s72\n\t"
    "s_and_b32 s17, s73, 0xffff\n\t"
    "s_mov_b32 s18, 6291456\n\t"
    "s_mov_b32 s19, 0x20000\n\t"
    "s_add_u32 m0, s53, 16384\n\t"
    "s_nop 0\n\t"
    "buffer_load_dwordx4 v2, s[4:7], s35 offen offset:0 lds\n\t"
    "s_add_u32 m0, s53, 17408\n\t"
    "s_nop 0\n\t"
    "buffer_load_dwordx4 v3, s[4:7], s35 offen offset:0 lds\n\t"
    "s_add_u32 m0, s53, 24448\n\t"
    "s_nop 0\n\t"
    "buffer_load_dwordx4 v2, s[4:7], s35 offen offset:128 lds\n\t"
    "s_add_u32 m0, s53, 25472\n\t"
    "s_nop 0\n\t"
    "buffer_load_dwordx4 v3, s[4:7], s35 offen offset:128 lds\n\t"
    "s_add_u32 m0, s52, 147456\n\t"
    "s_add_u32 s54, s76, s97\n\t"
    "buffer_load_dwordx4 v4, s[8:11], s54 offen offset:0 lds\n\t"
    "buffer_load_dwordx4 v5, s[8:11], s54 offen offset:1024 lds\n\t"
    "buffer_load_dwordx4 v6, s[8:11], s54 offen offset:2048 lds\n\t"
    "buffer_load_dwordx4 v7, s[8:11], s54 offen offset:3072 lds\n\t"
    "s_mov_b32 s12, s68\n\t"
    "s_and_b32 s13, s69, 0xffff\n\t"
    "s_mov_b32 s14, 1966080\n\t"
    "s_mov_b32 s15, 0x20000\n\t"
    "s_add_u32 s43, s29, 2\n\t"
    "s_sub_u32 s44, s43, 5\n\t"
    "s_cmp_ge_u32 s43, 5\n\t"
    "s_cselect_b32 s43, s44, s43\n\t"
    "s_lshl_b32 s44, s43, 8\n\t"
    "s_add_u32 s36, s46, s44\n\t"
    "s_mul_i32 s44, s43, 393216\n\t"
    "s_add_u32 s77, s47, s44\n\t"
    "s_add_u32 s43, s29, 3\n\t"
    "s_sub_u32 s44, s43, 5\n\t"
    "s_cmp_ge_u32 s43, 5\n\t"
    "s_cselect_b32 s43, s44, s43\n\t"
    "s_lshl_b32 s44, s43, 8\n\t"
    "s_add_u32 s37, s46, s44\n\t"
    "s_mul_i32 s44, s43, 393216\n\t"
    "s_add_u32 s72, s47, s44\n\t"
    "s_add_u32 s43, s29, 4\n\t"
    "s_sub_u32 s44, s43, 5\n\t"
    "s_cmp_ge_u32 s43, 5\n\t"
    "s_cselect_b32 s43, s44, s43\n\t"
    "s_lshl_b32 s44, s43, 8\n\t"
    "s_add_u32 s38, s46, s44\n\t"
    "s_mul_i32 s44, s43, 393216\n\t"
    "s_add_u32 s73, s47, s44\n\t"
    "v_add_u32 v8, v52, v51\n\t"
    "v_add_u32 v9, 9216, v8\n\t"
    "v_add_u32 v10, 18432, v8\n\t"
    "v_add_u32 v11, 27648, v8\n\t"
    "v_add_u32 v12, s52, v1\n\t"
    "s_mul_i32 s44, s41, 205\n\t"
    "s_lshr_b32 s44, s44, 10\n\t"
    "s_mul_i32 s45, s44, 5\n\t"
    "s_sub_u32 s45, s41, s45\n\t"
    "s_add_u32 s31, s44, s45\n\t"
    "s_and_b32 s31, s31, 3\n\t"
    "s_mul_i32 s43, s81, 5\n\t"
    "s_add_u32 s45, s45, s43\n\t"
    "s_mov_b32 s56, 3\n\t"
    "s_cmp_ge_u32 s44, 4\n\t"
    "s_cselect_b32 s56, 2, s56\n\t"
    "s_cmp_ge_u32 s44, 6\n\t"
    "s_cselect_b32 s56, 0, s56\n\t"
    "s_mul_i32 s43, s44, 3\n\t"
    "s_lshl_b32 s55, s44, 1\n\t"
    "s_add_u32 s55, s55, 4\n\t"
    "s_min_u32 s43, s43, s55\n\t"
    "s_lshl_b32 s55, s80, 4\n\t"
    "s_add_u32 s43, s43, s55\n\t"
    "s_mul_i32 s50, s43, 98304\n\t"
    "s_lshl_b32 s48, s45, 7\n\t"
    "s_add_u32 s43, s31, 0\n\t"
    "s_lshl_b32 s43, s43, 2\n\t"
    "s_add_u32 s43, s43, s30\n\t"
    "s_and_b32 s43, s43, 15\n\t"
    "s_lshr_b32 s44, s43, 1\n\t"
    "s_mul_i32 s44, s44, 12\n\t"
    "s_and_b32 s43, s43, 1\n\t"
    "s_lshl_b32 s43, s43, 1\n\t"
    "s_add_u32 s43, s43, s44\n\t"
    "s_lshl_b32 s64, s43, 10\n\t"
    "s_add_u32 s43, s31, 1\n\t"
    "s_lshl_b32 s43, s43, 2\n\t"
    "s_add_u32 s43, s43, s30\n\t"
    "s_and_b32 s43, s43, 15\n\t"
    "s_lshr_b32 s44, s43, 1\n\t"
    "s_mul_i32 s44, s44, 12\n\t"
    "s_and_b32 s43, s43, 1\n\t"
    "s_lshl_b32 s43, s43, 1\n\t"
    "s_add_u32 s43, s43, s44\n\t"
    "s_lshl_b32 s65, s43, 10\n\t"
    "s_add_u32 s43, s31, 2\n\t"
    "s_lshl_b32 s43, s43, 2\n\t"
    "s_add_u32 s43, s43, s30\n\t"
    "s_and_b32 s43, s43, 15\n\t"
    "s_lshr_b32 s44, s43, 1\n\t"
    "s_mul_i32 s44, s44, 12\n\t"
    "s_and_b32 s43, s43, 1\n\t"
    "s_lshl_b32 s43, s43, 1\n\t"
    "s_add_u32 s43, s43, s44\n\t"
    "s_lshl_b32 s66, s43, 10\n\t"
    "s_add_u32 s43, s31, 3\n\t"
    "s_lshl_b32 s43, s43, 2\n\t"
    "s_add_u32 s43, s43, s30\n\t"
    "s_and_b32 s43, s43, 15\n\t"
    "s_lshr_b32 s44, s43, 1\n\t"
    "s_mul_i32 s44, s44, 12\n\t"
    "s_and_b32 s43, s43, 1\n\t"
    "s_lshl_b32 s43, s43, 1\n\t"
    "s_add_u32 s43, s43, s44\n\t"
    "s_lshl_b32 s67, s43, 10\n\t"
    "s_waitcnt vmcnt(8)\n\t"
    "s_barrier\n\t"
    "s_add_u32 m0, s53, 32768\n\t"
    "s_nop 0\n\t"
    "buffer_load_dwordx4 v2, s[4:7], s36 offen offset:0 lds\n\t"
    "s_add_u32 m0, s53, 33792\n\t"
    "s_nop 0\n\t"
    "buffer_load_dwordx4 v3, s[4:7], s36 offen offset:0 lds\n\t"
    "s_add_u32 m0, s53, 40832\n\t"
    "s_nop 0\n\t"
    "buffer_load_dwordx4 v2, s[4:7], s36 offen offset:128 lds\n\t"
    "s_add_u32 m0, s53, 41856\n\t"
    "s_nop 0\n\t"
    "buffer_load_dwordx4 v3, s[4:7], s36 offen offset:128 lds\n\t"
    "s_add_u32 m0, s52, 131072\n\t"
    "s_add_u32 s54, s77, s97\n\t"
    "buffer_load_dwordx4 v4, s[8:11], s54 offen offset:0 lds\n\t"
    "buffer_load_dwordx4 v5, s[8:11], s54 offen offset:1024 lds\n\t"
    "buffer_load_dwordx4 v6, s[8:11], s54 offen offset:2048 lds\n\t"
    "buffer_load_dwordx4 v7, s[8:11], s54 offen offset:3072 lds\n\t"
    "s_waitcnt vmcnt(8)\n\t"
    "s_barrier\n\t"
    "s_add_u32 m0, s53, 49152\n\t"
    "s_nop 0\n\t"
    "buffer_load_dwordx4 v2, s[4:7], s37 offen offset:0 lds\n\t"
    "s_add_u32 m0, s53, 50176\n\t"
    "s_nop 0\n\t"
    "buffer_load_dwordx4 v3, s[4:7], s37 offen offset:0 lds\n\t"
    "s_add_u32 m0, s53, 57216\n\t"
    "s_nop 0\n\t"
    "buffer_load_dwordx4 v2, s[4:7], s37 offen offset:128 lds\n\t"
    "s_add_u32 m0, s53, 58240\n\t"
    "s_nop 0\n\t"
    "buffer_load_dwordx4 v3, s[4:7], s37 offen offset:128 lds\n\t"
    "s_add_u32 m0, s52, 0\n\t"
    "s_add_u32 s54, s72, s97\n\t"
    "buffer_load_dwordx4 v4, s[8:11], s54 offen offset:0 lds\n\t"
    "buffer_load_dwordx4 v5, s[8:11], s54 offen offset:1024 lds\n\t"
    "buffer_load_dwordx4 v6, s[8:11], s54 offen offset:2048 lds\n\t"
    "buffer_load_dwordx4 v7, s[8:11], s54 offen offset:3072 lds\n\t"
    "s_waitcnt vmcnt(8)\n\t"
    "s_barrier\n\t"
    "s_add_u32 m0, s53, 65536\n\t"
    "s_nop 0\n\t"
    "buffer_load_dwordx4 v2, s[4:7], s38 offen offset:0 lds\n\t"
    "s_add_u32 m0, s53, 66560\n\t"
    "s_nop 0\n\t"
    "buffer_load_dwordx4 v3, s[4:7], s38 offen offset:0 lds\n\t"
    "s_add_u32 m0, s53, 73600\n\t"
    "s_nop 0\n\t"
    "buffer_load_dwordx4 v2, s[4:7], s38 offen offset:128 lds\n\t"
    "s_add_u32 m0, s53, 74624\n\t"
    "s_nop 0\n\t"
    "buffer_load_dwordx4 v3, s[4:7], s38 offen offset:128 lds\n\t"
    "s_add_u32 m0, s52, 81920\n\t"
    "s_add_u32 s54, s73, s97\n\t"
    "buffer_load_dwordx4 v4, s[8:11], s54 offen offset:0 lds\n\t"
    "buffer_load_dwordx4 v5, s[8:11], s54 offen offset:1024 lds\n\t"
    "buffer_load_dwordx4 v6, s[8:11], s54 offen offset:2048 lds\n\t"
    "buffer_load_dwordx4 v7, s[8:11], s54 offen offset:3072 lds\n\t"
    "s_waitcnt vmcnt(8)\n\t"
    "s_barrier\n\t"
    "s_add_u32 m0, s52, 131072\n\t"
    "s_add_u32 s54, s39, s98\n\t"
    "buffer_load_dwordx4 v4, s[8:11], s54 offen offset:0 lds\n\t"
    "buffer_load_dwordx4 v5, s[8:11], s54 offen offset:1024 lds\n\t"
    "buffer_load_dwordx4 v6, s[8:11], s54 offen offset:2048 lds\n\t"
    "buffer_load_dwordx4 v7, s[8:11], s54 offen offset:3072 lds\n\t"
    "s_add_u32 m0, s52, 147456\n\t"
    "s_add_u32 s54, s39, s99\n\t"
    "buffer_load_dwordx4 v4, s[8:11], s54 offen offset:0 lds\n\t"
    "buffer_load_dwordx4 v5, s[8:11], s54 offen offset:1024 lds\n\t"
    "buffer_load_dwordx4 v6, s[8:11], s54 offen offset:2048 lds\n\t"
    "buffer_load_dwordx4 v7, s[8:11], s54 offen offset:3072 lds\n\t"
    "s_waitcnt vmcnt(8)\n\t"
    "s_barrier\n\t"
    "s_add_u32 m0, s52, 98304\n\t"
    "s_add_u32 s54, s76, s98\n\t"
    "buffer_load_dwordx4 v4, s[8:11], s54 offen offset:0 lds\n\t"
    "buffer_load_dwordx4 v5, s[8:11], s54 offen offset:1024 lds\n\t"
    "buffer_load_dwordx4 v6, s[8:11], s54 offen offset:2048 lds\n\t"
    "buffer_load_dwordx4 v7, s[8:11], s54 offen offset:3072 lds\n\t"
    "s_add_u32 m0, s52, 114688\n\t"
    "s_add_u32 s54, s77, s98\n\t"
    "buffer_load_dwordx4 v4, s[8:11], s54 offen offset:0 lds\n\t"
    "buffer_load_dwordx4 v5, s[8:11], s54 offen offset:1024 lds\n\t"
    "buffer_load_dwordx4 v6, s[8:11], s54 offen offset:2048 lds\n\t"
    "buffer_load_dwordx4 v7, s[8:11], s54 offen offset:3072 lds\n\t"
    "s_waitcnt vmcnt(8)\n\t"
    "s_barrier\n\t"
    "s_mul_i32 s53, s30, 6144\n\t"
    "s_add_u32 m0, s52, 8192\n\t"
    "s_add_u32 s54, s72, s98\n\t"
    "buffer_load_dwordx4 v4, s[8:11], s54 offen offset:0 lds\n\t"
    "buffer_load_dwordx4 v5, s[8:11], s54 offen offset:1024 lds\n\t"
    "buffer_load_dwordx4 v6, s[8:11], s54 offen offset:2048 lds\n\t"
    "buffer_load_dwordx4 v7, s[8:11], s54 offen offset:3072 lds\n\t"
    "s_add_u32 m0, s52, 24576\n\t"
    "s_add_u32 s54, s73, s98\n\t"
    "buffer_load_dwordx4 v4, s[8:11], s54 offen offset:0 lds\n\t"
    "buffer_load_dwordx4 v5, s[8:11], s54 offen offset:1024 lds\n\t"
    "buffer_load_dwordx4 v6, s[8:11], s54 offen offset:2048 lds\n\t"
    "buffer_load_dwordx4 v7, s[8:11], s54 offen offset:3072 lds\n\t"
    "s_waitcnt vmcnt(8)\n\t"
    "s_barrier\n\t"
    "s_add_u32 m0, s52, 81920\n\t"
    "s_add_u32 s54, s76, s99\n\t"
    "buffer_load_dwordx4 v4, s[8:11], s54 offen offset:0 lds\n\t"
    "buffer_load_dwordx4 v5, s[8:11], s54 offen offset:1024 lds\n\t"
    "buffer_load_dwordx4 v6, s[8:11], s54 offen offset:2048 lds\n\t"
    "buffer_load_dwordx4 v7, s[8:11], s54 offen offset:3072 lds\n\t"
    "s_add_u32 m0, s52, 147456\n\t"
    "s_add_u32 s54, s77, s99\n\t"
    "buffer_load_dwordx4 v4, s[8:11], s54 offen offset:0 lds\n\t"
    "buffer_load_dwordx4 v5, s[8:11], s54 offen offset:1024 lds\n\t"
    "buffer_load_dwordx4 v6, s[8:11], s54 offen offset:2048 lds\n\t"
    "buffer_load_dwordx4 v7, s[8:11], s54 offen offset:3072 lds\n\t"
    "s_waitcnt vmcnt(8)\n\t"
    "s_barrier\n\t"
    "s_add_u32 m0, s52, 65536\n\t"
    "s_add_u32 s54, s72, s99\n\t"
    "buffer_load_dwordx4 v4, s[8:11], s54 offen offset:0 lds\n\t"
    "buffer_load_dwordx4 v5, s[8:11], s54 offen offset:1024 lds\n\t"
    "buffer_load_dwordx4 v6, s[8:11], s54 offen offset:2048 lds\n\t"
    "buffer_load_dwordx4 v7, s[8:11], s54 offen offset:3072 lds\n\t"
    "s_add_u32 m0, s52, 49152\n\t"
    "s_add_u32 s54, s73, s99\n\t"
    "buffer_load_dwordx4 v4, s[8:11], s54 offen offset:0 lds\n\t"
    "buffer_load_dwordx4 v5, s[8:11], s54 offen offset:1024 lds\n\t"
    "buffer_load_dwordx4 v6, s[8:11], s54 offen offset:2048 lds\n\t"
    "buffer_load_dwordx4 v7, s[8:11], s54 offen offset:3072 lds\n\t"
    "s_waitcnt vmcnt(8)\n\t"
    "s_barrier\n\t"
    "s_lshl_b32 s54, s97, 5\n\t"
    "s_add_u32 s54, s54, s64\n\t"
    "s_mul_i32 s54, s54, 20\n\t"
    "s_add_u32 m0, s52, 0\n\t"
    "s_add_u32 s54, s54, s48\n\t"
    "buffer_load_dwordx4 v8, s[12:15], s54 offen offset:0 lds\n\t"
    "buffer_load_dwordx4 v9, s[12:15], s54 offen offset:1024 lds\n\t"
    "buffer_load_dwordx4 v10, s[12:15], s54 offen offset:2048 lds\n\t"
    "buffer_load_dwordx4 v11, s[12:15], s54 offen offset:3072 lds\n\t"
    "s_lshl_b32 s54, s97, 5\n\t"
    "s_add_u32 s54, s54, s65\n\t"
    "s_mul_i32 s54, s54, 20\n\t"
    "s_add_u32 m0, s52, 32768\n\t"
    "s_add_u32 s54, s54, s48\n\t"
    "buffer_load_dwordx4 v8, s[12:15], s54 offen offset:0 lds\n\t"
    "buffer_load_dwordx4 v9, s[12:15], s54 offen offset:1024 lds\n\t"
    "buffer_load_dwordx4 v10, s[12:15], s54 offen offset:2048 lds\n\t"
    "buffer_load_dwordx4 v11, s[12:15], s54 offen offset:3072 lds\n\t"
    "s_lshl_b32 s54, s97, 5\n\t"
    "s_add_u32 s54, s54, s66\n\t"
    "s_mul_i32 s54, s54, 20\n\t"
    "s_add_u32 m0, s52, 122880\n\t"
    "s_add_u32 s54, s54, s48\n\t"
    "buffer_load_dwordx4 v8, s[12:15], s54 offen offset:0 lds\n\t"
    "buffer_load_dwordx4 v9, s[12:15], s54 offen offset:1024 lds\n\t"
    "buffer_load_dwordx4 v10, s[12:15], s54 offen offset:2048 lds\n\t"
    "buffer_load_dwordx4 v11, s[12:15], s54 offen offset:3072 lds\n\t"
    "s_waitcnt vmcnt(12)\n\t"
    "s_barrier\n\t"
    "s_lshl_b32 s54, s97, 5\n\t"
    "s_add_u32 s54, s54, s67\n\t"
    "s_mul_i32 s54, s54, 20\n\t"
    "s_add_u32 m0, s52, 16384\n\t"
    "s_add_u32 s54, s54, s48\n\t"
    "buffer_load_dwordx4 v8, s[12:15], s54 offen offset:0 lds\n\t"
    "buffer_load_dwordx4 v9, s[12:15], s54 offen offset:1024 lds\n\t"
    "buffer_load_dwordx4 v10, s[12:15], s54 offen offset:2048 lds\n\t"
    "buffer_load_dwordx4 v11, s[12:15], s54 offen offset:3072 lds\n\t"
    "s_lshl_b32 s54, s98, 5\n\t"
    "s_add_u32 s54, s54, s64\n\t"
    "s_mul_i32 s54, s54, 20\n\t"
    "s_add_u32 m0, s52, 81920\n\t"
    "s_add_u32 s54, s54, s48\n\t"
    "buffer_load_dwordx4 v8, s[12:15], s54 offen offset:0 lds\n\t"
    "buffer_load_dwordx4 v9, s[12:15], s54 offen offset:1024 lds\n\t"
    "buffer_load_dwordx4 v10, s[12:15], s54 offen offset:2048 lds\n\t"
    "buffer_load_dwordx4 v11, s[12:15], s54 offen offset:3072 lds\n\t"
    "s_barrier\n\t"
    "s_cmp_eq_u32 s56, 0\n\t"
    "s_cbranch_scc1 L_lspare_%=\n\t"
    "s_cmp_eq_u32 s56, 2\n\t"
    "s_cbranch_scc1 L_lng2_%=\n\t"
    "s_lshl_b32 s54, s97, 5\n\t"
    "s_add_u32 s54, s54, s64\n\t"
    "s_add_u32 s54, s54, s50\n\t"
    "s_add_u32 m0, s53, 139264\n\t"
    "s_nop 0\n\t"
    "buffer_load_dwordx4 v1, s[16:19], s54 offen offset:0 sc1 lds\n\t"
    "buffer_load_dwordx4 v1, s[16:19], s54 offen offset:1024 sc1 lds\n\t"
    "s_add_u32 m0, s53, 141312\n\t"
    "s_add_u32 s54, s54, 98304\n\t"
    "buffer_load_dwordx4 v1, s[16:19], s54 offen offset:0 sc1 lds\n\t"
    "buffer_load_dwordx4 v1, s[16:19], s54 offen offset:1024 sc1 lds\n\t"
    "s_add_u32 m0, s53, 143360\n\t"
    "s_add_u32 s54, s54, 98304\n\t"
    "buffer_load_dwordx4 v1, s[16:19], s54 offen offset:0 sc1 lds\n\t"
    "buffer_load_dwordx4 v1, s[16:19], s54 offen offset:1024 sc1 lds\n\t"
    "s_barrier\n\t"
    "s_lshl_b32 s54, s97, 5\n\t"
    "s_add_u32 s54, s54, s65\n\t"
    "s_add_u32 s54, s54, s50\n\t"
    "s_add_u32 m0, s53, 49152\n\t"
    "s_nop 0\n\t"
    "buffer_load_dwordx4 v1, s[16:19], s54 offen offset:0 sc1 lds\n\t"
    "buffer_load_dwordx4 v1, s[16:19], s54 offen offset:1024 sc1 lds\n\t"
    "s_add_u32 m0, s53, 51200\n\t"
    "s_add_u32 s54, s54, 98304\n\t"
    "buffer_load_dwordx4 v1, s[16:19], s54 offen offset:0 sc1 lds\n\t"
    "buffer_load_dwordx4 v1, s[16:19], s54 offen offset:1024 sc1 lds\n\t"
    "s_add_u32 m0, s53, 53248\n\t"
    "s_add_u32 s54, s54, 98304\n\t"
    "buffer_load_dwordx4 v1, s[16:19], s54 offen offset:0 sc1 lds\n\t"
    "buffer_load_dwordx4 v1, s[16:19], s54 offen offset:1024 sc1 lds\n\t"
    "s_waitcnt vmcnt(6)\n\t"
    "s_barrier\n\t"
    "s_lshl_b32 s54, s97, 5\n\t"
    "s_add_u32 s54, s54, s66\n\t"
    "s_add_u32 s54, s54, s50\n\t"
    "s_add_u32 m0, s53, 98304\n\t"
    "s_nop 0\n\t"
    "buffer_load_dwordx4 v1, s[16:19], s54 offen offset:0 sc1 lds\n\t"
    "buffer_load_dwordx4 v1, s[16:19], s54 offen offset:1024 sc1 lds\n\t"
    "s_add_u32 m0, s53, 100352\n\t"
    "s_add_u32 s54, s54, 98304\n\t"
    "buffer_load_dwordx4 v1, s[16:19], s54 offen offset:0 sc1 lds\n\t"
    "buffer_load_dwordx4 v1, s[16:19], s54 offen offset:1024 sc1 lds\n\t"
    "s_add_u32 m0, s53, 102400\n\t"
    "s_add_u32 s54, s54, 98304\n\t"
    "buffer_load_dwordx4 v1, s[16:19], s54 offen offset:0 sc1 lds\n\t"
    "buffer_load_dwordx4 v1, s[16:19], s54 offen offset:1024 sc1 lds\n\t"
    "s_waitcnt vmcnt(6)\n\t"
    "s_barrier\n\t"
    "s_lshl_b32 s54, s97, 5\n\t"
    "s_add_u32 s54, s54, s67\n\t"
    "s_add_u32 s54, s54, s50\n\t"
    "s_add_u32 m0, s53, 139264\n\t"
    "s_nop 0\n\t"
    "buffer_load_dwordx4 v1, s[16:19], s54 offen offset:0 sc1 lds\n\t"
    "buffer_load_dwordx4 v1, s[16:19], s54 offen offset:1024 sc1 lds\n\t"
    "s_add_u32 m0, s53, 141312\n\t"
    "s_add_u32 s54, s54, 98304\n\t"
    "buffer_load_dwordx4 v1, s[16:19], s54 offen offset:0 sc1 lds\n\t"
    "buffer_load_dwordx4 v1, s[16:19], s54 offen offset:1024 sc1 lds\n\t"
    "s_add_u32 m0, s53, 143360\n\t"
    "s_add_u32 s54, s54, 98304\n\t"
    "buffer_load_dwordx4 v1, s[16:19], s54 offen offset:0 sc1 lds\n\t"
    "buffer_load_dwordx4 v1, s[16:19], s54 offen offset:1024 sc1 lds\n\t"
    "s_lshl_b32 s54, s98, 5\n\t"
    "s_add_u32 s54, s54, s65\n\t"
    "s_mul_i32 s54, s54, 20\n\t"
    "s_add_u32 m0, s52, 0\n\t"
    "s_add_u32 s54, s54, s48\n\t"
    "buffer_load_dwordx4 v8, s[12:15], s54 offen offset:0 lds\n\t"
    "buffer_load_dwordx4 v9, s[12:15], s54 offen offset:1024 lds\n\t"
    "buffer_load_dwordx4 v10, s[12:15], s54 offen offset:2048 lds\n\t"
    "buffer_load_dwordx4 v11, s[12:15], s54 offen offset:3072 lds\n\t"
    "s_waitcnt vmcnt(10)\n\t"
    "s_barrier\n\t"
    "s_lshl_b32 s54, s98, 5\n\t"
    "s_add_u32 s54, s54, s64\n\t"
    "s_add_u32 s54, s54, s50\n\t"
    "s_add_u32 m0, s53, 32768\n\t"
    "s_nop 0\n\t"
    "buffer_load_dwordx4 v1, s[16:19], s54 offen offset:0 sc1 lds\n\t"
    "buffer_load_dwordx4 v1, s[16:19], s54 offen offset:1024 sc1 lds\n\t"
    "s_add_u32 m0, s53, 34816\n\t"
    "s_add_u32 s54, s54, 98304\n\t"
    "buffer_load_dwordx4 v1, s[16:19], s54 offen offset:0 sc1 lds\n\t"
    "buffer_load_dwordx4 v1, s[16:19], s54 offen offset:1024 sc1 lds\n\t"
    "s_add_u32 m0, s53, 36864\n\t"
    "s_add_u32 s54, s54, 98304\n\t"
    "buffer_load_dwordx4 v1, s[16:19], s54 offen offset:0 sc1 lds\n\t"
    "buffer_load_dwordx4 v1, s[16:19], s54 offen offset:1024 sc1 lds\n\t"
    "s_lshl_b32 s54, s98, 5\n\t"
    "s_add_u32 s54, s54, s66\n\t"
    "s_mul_i32 s54, s54, 20\n\t"
    "s_add_u32 m0, s52, 57344\n\t"
    "s_add_u32 s54, s54, s48\n\t"
    "buffer_load_dwordx4 v8, s[12:15], s54 offen offset:0 lds\n\t"
    "buffer_load_dwordx4 v9, s[12:15], s54 offen offset:1024 lds\n\t"
    "buffer_load_dwordx4 v10, s[12:15], s54 offen offset:2048 lds\n\t"
    "buffer_load_dwordx4 v11, s[12:15], s54 offen offset:3072 lds\n\t"
    "s_waitcnt vmcnt(14)\n\t"
    "s_barrier\n\t"
    "s_lshl_b32 s54, s98, 5\n\t"
    "s_add_u32 s54, s54, s65\n\t"
    "s_add_u32 s54, s54, s50\n\t"
    "s_add_u32 m0, s53, 98304\n\t"
    "s_nop 0\n\t"
    "buffer_load_dwordx4 v1, s[16:19], s54 offen offset:0 sc1 lds\n\t"
    "buffer_load_dwordx4 v1, s[16:19], s54 offen offset:1024 sc1 lds\n\t"
    "s_add_u32 m0, s53, 100352\n\t"
    "s_add_u32 s54, s54, 98304\n\t"
    "buffer_load_dwordx4 v1, s[16:19], s54 offen offset:0 sc1 lds\n\t"
    "buffer_load_dwordx4 v1, s[16:19], s54 offen offset:1024 sc1 lds\n\t"
    "s_add_u32 m0, s53, 102400\n\t"
    "s_add_u32 s54, s54, 98304\n\t"
    "buffer_load_dwordx4 v1, s[16:19], s54 offen offset:0 sc1 lds\n\t"
    "buffer_load_dwordx4 v1, s[16:19], s54 offen offset:1024 sc1 lds\n\t"
    "s_lshl_b32 s54, s98, 5\n\t"
    "s_add_u32 s54, s54, s67\n\t"
    "s_mul_i32 s54, s54, 20\n\t"
    "s_add_u32 m0, s52, 122880\n\t"
    "s_add_u32 s54, s54, s48\n\t"
    "buffer_load_dwordx4 v8, s[12:15], s54 offen offset:0 lds\n\t"
    "buffer_load_dwordx4 v9, s[12:15], s54 offen offset:1024 lds\n\t"
    "buffer_load_dwordx4 v10, s[12:15], s54 offen offset:2048 lds\n\t"
    "buffer_load_dwordx4 v11, s[12:15], s54 offen offset:3072 lds\n\t"
    "s_waitcnt vmcnt(14)\n\t"
    "s_barrier\n\t"
    "s_lshl_b32 s54, s98, 5\n\t"
    "s_add_u32 s54, s54, s66\n\t"
    "s_add_u32 s54, s54, s50\n\t"
    "s_add_u32 m0, s53, 139264\n\t"
    "s_nop 0\n\t"
    "buffer_load_dwordx4 v1, s[16:19], s54 offen offset:0 sc1 lds\n\t"
    "buffer_load_dwordx4 v1, s[16:19], s54 offen offset:1024 sc1 lds\n\t"
    "s_add_u32 m0, s53, 141312\n\t"
    "s_add_u32 s54, s54, 98304\n\t"
    "buffer_load_dwordx4 v1, s[16:19], s54 offen offset:0 sc1 lds\n\t"
    "buffer_load_dwordx4 v1, s[16:19], s54 offen offset:1024 sc1 lds\n\t"
    "s_add_u32 m0, s53, 143360\n\t"
    "s_add_u32 s54, s54, 98304\n\t"
    "buffer_load_dwordx4 v1, s[16:19], s54 offen offset:0 sc1 lds\n\t"
    "buffer_load_dwordx4 v1, s[16:19], s54 offen offset:1024 sc1 lds\n\t"
    "s_lshl_b32 s54, s99, 5\n\t"
    "s_add_u32 s54, s54, s64\n\t"
    "s_mul_i32 s54, s54, 20\n\t"
    "s_add_u32 m0, s52, 16384\n\t"
    "s_add_u32 s54, s54, s48\n\t"
    "buffer_load_dwordx4 v8, s[12:15], s54 offen offset:0 lds\n\t"
    "buffer_load_dwordx4 v9, s[12:15], s54 offen offset:1024 lds\n\t"
    "buffer_load_dwordx4 v10, s[12:15], s54 offen offset:2048 lds\n\t"
    "buffer_load_dwordx4 v11, s[12:15], s54 offen offset:3072 lds\n\t"
    "s_waitcnt vmcnt(14)\n\t"
    "s_barrier\n\t"
    "s_lshl_b32 s54, s98, 5\n\t"
    "s_add_u32 s54, s54, s67\n\t"
    "s_add_u32 s54, s54, s50\n\t"
    "s_add_u32 m0, s53, 32768\n\t"
    "s_nop 0\n\t"
    "buffer_load_dwordx4 v1, s[16:19], s54 offen offset:0 sc1 lds\n\t"
    "buffer_load_dwordx4 v1, s[16:19], s54 offen offset:1024 sc1 lds\n\t"
    "s_add_u32 m0, s53, 34816\n\t"
    "s_add_u32 s54, s54, 98304\n\t"
    "buffer_load_dwordx4 v1, s[16:19], s54 offen offset:0 sc1 lds\n\t"
    "buffer_load_dwordx4 v1, s[16:19], s54 offen offset:1024 sc1 lds\n\t"
    "s_add_u32 m0, s53, 36864\n\t"
    "s_add_u32 s54, s54, 98304\n\t"
    "buffer_load_dwordx4 v1, s[16:19], s54 offen offset:0 sc1 lds\n\t"
    "buffer_load_dwordx4 v1, s[16:19], s54 offen offset:1024 sc1 lds\n\t"
    "s_lshl_b32 s54, s99, 5\n\t"
    "s_add_u32 s54, s54, s65\n\t"
    "s_mul_i32 s54, s54, 20\n\t"
    "s_add_u32 m0, s52, 73728\n\t"
    "s_add_u32 s54, s54, s48\n\t"
    "buffer_load_dwordx4 v8, s[12:15], s54 offen offset:0 lds\n\t"
    "buffer_load_dwordx4 v9, s[12:15], s54 offen offset:1024 lds\n\t"
    "buffer_load_dwordx4 v10, s[12:15], s54 offen offset:2048 lds\n\t"
    "buffer_load_dwordx4 v11, s[12:15], s54 offen offset:3072 lds\n\t"
    "s_waitcnt vmcnt(14)\n\t"
    "s_barrier\n\t"
    "s_lshl_b32 s54, s99, 5\n\t"
    "s_add_u32 s54, s54, s64\n\t"
    "s_add_u32 s54, s54, s50\n\t"
    "s_add_u32 m0, s53, 98304\n\t"
    "s_nop 0\n\t"
    "buffer_load_dwordx4 v1, s[16:19], s54 offen offset:0 sc1 lds\n\t"
    "buffer_load_dwordx4 v1, s[16:19], s54 offen offset:1024 sc1 lds\n\t"
    "s_add_u32 m0, s53, 100352\n\t"
    "s_add_u32 s54, s54, 98304\n\t"
    "buffer_load_dwordx4 v1, s[16:19], s54 offen offset:0 sc1 lds\n\t"
    "buffer_load_dwordx4 v1, s[16:19], s54 offen offset:1024 sc1 lds\n\t"
    "s_add_u32 m0, s53, 102400\n\t"
    "s_add_u32 s54, s54, 98304\n\t"
    "buffer_load_dwordx4 v1, s[16:19], s54 offen offset:0 sc1 lds\n\t"
    "buffer_load_dwordx4 v1, s[16:19], s54 offen offset:1024 sc1 lds\n\t"
    "s_lshl_b32 s54, s99, 5\n\t"
    "s_add_u32 s54, s54, s66\n\t"
    "s_mul_i32 s54, s54, 20\n\t"
    "s_add_u32 m0, s52, 0\n\t"
    "s_add_u32 s54, s54, s48\n\t"
    "buffer_load_dwordx4 v8, s[12:15], s54 offen offset:0 lds\n\t"
    "buffer_load_dwordx4 v9, s[12:15], s54 offen offset:1024 lds\n\t"
    "buffer_load_dwordx4 v10, s[12:15], s54 offen offset:2048 lds\n\t"
    "buffer_load_dwordx4 v11, s[12:15], s54 offen offset:3072 lds\n\t"
    "s_waitcnt vmcnt(14)\n\t"
    "s_barrier\n\t"
    "s_lshl_b32 s54, s99, 5\n\t"
    "s_add_u32 s54, s54, s65\n\t"
    "s_add_u32 s54, s54, s50\n\t"
    "s_add_u32 m0, s53, 139264\n\t"
    "s_nop 0\n\t"
    "buffer_load_dwordx4 v1, s[16:19], s54 offen offset:0 sc1 lds\n\t"
    "buffer_load_dwordx4 v1, s[16:19], s54 offen offset:1024 sc1 lds\n\t"
    "s_add_u32 m0, s53, 141312\n\t"
    "s_add_u32 s54, s54, 98304\n\t"
    "buffer_load_dwordx4 v1, s[16:19], s54 offen offset:0 sc1 lds\n\t"
    "buffer_load_dwordx4 v1, s[16:19], s54 offen offset:1024 sc1 lds\n\t"
    "s_add_u32 m0, s53, 143360\n\t"
    "s_add_u32 s54, s54, 98304\n\t"
    "buffer_load_dwordx4 v1, s[16:19], s54 offen offset:0 sc1 lds\n\t"
    "buffer_load_dwordx4 v1, s[16:19], s54 offen offset:1024 sc1 lds\n\t"
    "s_lshl_b32 s54, s99, 5\n\t"
    "s_add_u32 s54, s54, s67\n\t"
    "s_mul_i32 s54, s54, 20\n\t"
    "s_add_u32 m0, s52, 57344\n\t"
    "s_add_u32 s54, s54, s48\n\t"
    "buffer_load_dwordx4 v8, s[12:15], s54 offen offset:0 lds\n\t"
    "buffer_load_dwordx4 v9, s[12:15], s54 offen offset:1024 lds\n\t"
    "buffer_load_dwordx4 v10, s[12:15], s54 offen offset:2048 lds\n\t"
    "buffer_load_dwordx4 v11, s[12:15], s54 offen offset:3072 lds\n\t"
    "s_waitcnt vmcnt(14)\n\t"
    "s_barrier\n\t"
    "s_lshl_b32 s54, s99, 5\n\t"
    "s_add_u32 s54, s54, s66\n\t"
    "s_add_u32 s54, s54, s50\n\t"
    "s_add_u32 m0, s53, 32768\n\t"
    "s_nop 0\n\t"
    "buffer_load_dwordx4 v1, s[16:19], s54 offen offset:0 sc1 lds\n\t"
    "buffer_load_dwordx4 v1, s[16:19], s54 offen offset:1024 sc1 lds\n\t"
    "s_add_u32 m0, s53, 34816\n\t"
    "s_add_u32 s54, s54, 98304\n\t"
    "buffer_load_dwordx4 v1, s[16:19], s54 offen offset:0 sc1 lds\n\t"
    "buffer_load_dwordx4 v1, s[16:19], s54 offen offset:1024 sc1 lds\n\t"
    "s_add_u32 m0, s53, 36864\n\t"
    "s_add_u32 s54, s54, 98304\n\t"
    "buffer_load_dwordx4 v1, s[16:19], s54 offen offset:0 sc1 lds\n\t"
    "buffer_load_dwordx4 v1, s[16:19], s54 offen offset:1024 sc1 lds\n\t"
    "s_waitcnt vmcnt(10)\n\t"
    "s_barrier\n\t"
    "s_lshl_b32 s54, s99, 5\n\t"
    "s_add_u32 s54, s54, s67\n\t"
    "s_add_u32 s54, s54, s50\n\t"
    "s_add_u32 m0, s53, 90112\n\t"
    "s_nop 0\n\t"
    "buffer_load_dwordx4 v1, s[16:19], s54 offen offset:0 sc1 lds\n\t"
    "buffer_load_dwordx4 v1, s[16:19], s54 offen offset:1024 sc1 lds\n\t"
    "s_add_u32 m0, s53, 92160\n\t"
    "s_add_u32 s54, s54, 98304\n\t"
    "buffer_load_dwordx4 v1, s[16:19], s54 offen offset:0 sc1 lds\n\t"
    "buffer_load_dwordx4 v1, s[16:19], s54 offen offset:1024 sc1 lds\n\t"
    "s_add_u32 m0, s53, 94208\n\t"
    "s_add_u32 s54, s54, 98304\n\t"
    "buffer_load_dwordx4 v1, s[16:19], s54 offen offset:0 sc1 lds\n\t"
    "buffer_load_dwordx4 v1, s[16:19], s54 offen offset:1024 sc1 lds\n\t"
    "s_waitcnt vmcnt(6)\n\t"
    "s_barrier\n\t"
    "s_waitcnt vmcnt(0)\n\t"
    "s_barrier\n\t"
    "s_barrier\n\t"
    "s_branch L_end_%=\n\t"
    "L_lng2_%=:\n\t"
    "s_lshl_b32 s54, s97, 5\n\t"
    "s_add_u32 s54, s54, s64\n\t"
    "s_add_u32 s54, s54, s50\n\t"
    "s_add_u32 m0, s53, 139264\n\t"
    "s_nop 0\n\t"
    "buffer_load_dwordx4 v1, s[16:19], s54 offen offset:0 sc1 lds\n\t"
    "buffer_load_dwordx4 v1, s[16:19], s54 offen offset:1024 sc1 lds\n\t"
    "s_add_u32 m0, s53, 141312\n\t"
    "s_add_u32 s54, s54, 98304\n\t"
    "buffer_load_dwordx4 v1, s[16:19], s54 offen offset:0 sc1 lds\n\t"
    "buffer_load_dwordx4 v1, s[16:19], s54 offen offset:1024 sc1 lds\n\t"
    "s_barrier\n\t"
    "s_lshl_b32 s54, s97, 5\n\t"
    "s_add_u32 s54, s54, s65\n\t"
    "s_add_u32 s54, s54, s50\n\t"
    "s_add_u32 m0, s53, 49152\n\t"
    "s_nop 0\n\t"
    "buffer_load_dwordx4 v1, s[16:19], s54 offen offset:0 sc1 lds\n\t"
    "buffer_load_dwordx4 v1, s[16:19], s54 offen offset:1024 sc1 lds\n\t"
    "s_add_u32 m0, s53, 51200\n\t"
    "s_add_u32 s54, s54, 98304\n\t"
    "buffer_load_dwordx4 v1, s[16:19], s54 offen offset:0 sc1 lds\n\t"
    "buffer_load_dwordx4 v1, s[16:19], s54 offen offset:1024 sc1 lds\n\t"
    "s_waitcnt vmcnt(4)\n\t"
    "s_barrier\n\t"
    "s_lshl_b32 s54, s97, 5\n\t"
    "s_add_u32 s54, s54, s66\n\t"
    "s_add_u32 s54, s54, s50\n\t"
    "s_add_u32 m0, s53, 98304\n\t"
    "s_nop 0\n\t"
    "buffer_load_dwordx4 v1, s[16:19], s54 offen offset:0 sc1 lds\n\t"
    "buffer_load_dwordx4 v1, s[16:19], s54 offen offset:1024 sc1 lds\n\t"
    "s_add_u32 m0, s53, 100352\n\t"
    "s_add_u32 s54, s54, 98304\n\t"
    "buffer_load_dwordx4 v1, s[16:19], s54 offen offset:0 sc1 lds\n\t"
    "buffer_load_dwordx4 v1, s[16:19], s54 offen offset:1024 sc1 lds\n\t"
    "s_waitcnt vmcnt(4)\n\t"
    "s_barrier\n\t"
    "s_lshl_b32 s54, s97, 5\n\t"
    "s_add_u32 s54, s54, s67\n\t"
    "s_add_u32 s54, s54, s50\n\t"
    "s_add_u32 m0, s53, 139264\n\t"
    "s_nop 0\n\t"
    "buffer_load_dwordx4 v1, s[16:19], s54 offen offset:0 sc1 lds\n\t"
    "buffer_load_dwordx4 v1, s[16:19], s54 offen offset:1024 sc1 lds\n\t"
    "s_add_u32 m0, s53, 141312\n\t"
    "s_add_u32 s54, s54, 98304\n\t"
    "buffer_load_dwordx4 v1, s[16:19], s54 offen offset:0 sc1 lds\n\t"
    "buffer_load_dwordx4 v1, s[16:19], s54 offen offset:1024 sc1 lds\n\t"
    "s_lshl_b32 s54, s98, 5\n\t"
    "s_add_u32 s54, s54, s65\n\t"
    "s_mul_i32 s54, s54, 20\n\t"
    "s_add_u32 m0, s52, 0\n\t"
    "s_add_u32 s54, s54, s48\n\t"
    "buffer_load_dwordx4 v8, s[12:15], s54 offen offset:0 lds\n\t"
    "buffer_load_dwordx4 v9, s[12:15], s54 offen offset:1024 lds\n\t"
    "buffer_load_dwordx4 v10, s[12:15], s54 offen offset:2048 lds\n\t"
    "buffer_load_dwordx4 v11, s[12:15], s54 offen offset:3072 lds\n\t"
    "s_waitcnt vmcnt(8)\n\t"
    "s_barrier\n\t"
    "s_lshl_b32 s54, s98, 5\n\t"
    "s_add_u32 s54, s54, s64\n\t"
    "s_add_u32 s54, s54, s50\n\t"
    "s_add_u32 m0, s53, 32768\n\t"
    "s_nop 0\n\t"
    "buffer_load_dwordx4 v1, s[16:19], s54 offen offset:0 sc1 lds\n\t"
    "buffer_load_dwordx4 v1, s[16:19], s54 offen offset:1024 sc1 lds\n\t"
    "s_add_u32 m0, s53, 34816\n\t"
    "s_add_u32 s54, s54, 98304\n\t"
    "buffer_load_dwordx4 v1, s[16:19], s54 offen offset:0 sc1 lds\n\t"
    "buffer_load_dwordx4 v1, s[16:19], s54 offen offset:1024 sc1 lds\n\t"
    "s_lshl_b32 s54, s98, 5\n\t"
    "s_add_u32 s54, s54, s66\n\t"
    "s_mul_i32 s54, s54, 20\n\t"
    "s_add_u32 m0, s52, 57344\n\t"
    "s_add_u32 s54, s54, s48\n\t"
    "buffer_load_dwordx4 v8, s[12:15], s54 offen offset:0 lds\n\t"
    "buffer_load_dwordx4 v9, s[12:15], s54 offen offset:1024 lds\n\t"
    "buffer_load_dwordx4 v10, s[12:15], s54 offen offset:2048 lds\n\t"
    "buffer_load_dwordx4 v11, s[12:15], s54 offen offset:3072 lds\n\t"
    "s_waitcnt vmcnt(12)\n\t"
    "s_barrier\n\t"
    "s_lshl_b32 s54, s98, 5\n\t"
    "s_add_u32 s54, s54, s65\n\t"
    "s_add_u32 s54, s54, s50\n\t"
    "s_add_u32 m0, s53, 98304\n\t"
    "s_nop 0\n\t"
    "buffer_load_dwordx4 v1, s[16:19], s54 offen offset:0 sc1 lds\n\t"
    "buffer_load_dwordx4 v1, s[16:19], s54 offen offset:1024 sc1 lds\n\t"
    "s_add_u32 m0, s53, 100352\n\t"
    "s_add_u32 s54, s54, 98304\n\t"
    "buffer_load_dwordx4 v1, s[16:19], s54 offen offset:0 sc1 lds\n\t"
    "buffer_load_dwordx4 v1, s[16:19], s54 offen offset:1024 sc1 lds\n\t"
    "s_lshl_b32 s54, s98, 5\n\t"
    "s_add_u32 s54, s54, s67\n\t"
    "s_mul_i32 s54, s54, 20\n\t"
    "s_add_u32 m0, s52, 122880\n\t"
    "s_add_u32 s54, s54, s48\n\t"
    "buffer_load_dwordx4 v8, s[12:15], s54 offen offset:0 lds\n\t"
    "buffer_load_dwordx4 v9, s[12:15], s54 offen offset:1024 lds\n\t"
    "buffer_load_dwordx4 v10, s[12:15], s54 offen offset:2048 lds\n\t"
    "buffer_load_dwordx4 v11, s[12:15], s54 offen offset:3072 lds\n\t"
    "s_waitcnt vmcnt(12)\n\t"
    "s_barrier\n\t"
    "s_lshl_b32 s54, s98, 5\n\t"
    "s_add_u32 s54, s54, s66\n\t"
    "s_add_u32 s54, s54, s50\n\t"
    "s_add_u32 m0, s53, 139264\n\t"
    "s_nop 0\n\t"
    "buffer_load_dwordx4 v1, s[16:19], s54 offen offset:0 sc1 lds\n\t"
    "buffer_load_dwordx4 v1, s[16:19], s54 offen offset:1024 sc1 lds\n\t"
    "s_add_u32 m0, s53, 141312\n\t"
    "s_add_u32 s54, s54, 98304\n\t"
    "buffer_load_dwordx4 v1, s[16:19], s54 offen offset:0 sc1 lds\n\t"
    "buffer_load_dwordx4 v1, s[16:19], s54 offen offset:1024 sc1 lds\n\t"
    "s_lshl_b32 s54, s99, 5\n\t"
    "s_add_u32 s54, s54, s64\n\t"
    "s_mul_i32 s54, s54, 20\n\t"
    "s_add_u32 m0, s52, 16384\n\t"
    "s_add_u32 s54, s54, s48\n\t"
    "buffer_load_dwordx4 v8, s[12:15], s54 offen offset:0 lds\n\t"
    "buffer_load_dwordx4 v9, s[12:15], s54 offen offset:1024 lds\n\t"
    "buffer_load_dwordx4 v10, s[12:15], s54 offen offset:2048 lds\n\t"
    "buffer_load_dwordx4 v11, s[12:15], s54 offen offset:3072 lds\n\t"
    "s_waitcnt vmcnt(12)\n\t"
    "s_barrier\n\t"
    "s_lshl_b32 s54, s98, 5\n\t"
    "s_add_u32 s54, s54, s67\n\t"
    "s_add_u32 s54, s54, s50\n\t"
    "s_add_u32 m0, s53, 32768\n\t"
    "s_nop 0\n\t"
    "buffer_load_dwordx4 v1, s[16:19], s54 offen offset:0 sc1 lds\n\t"
    "buffer_load_dwordx4 v1, s[16:19], s54 offen offset:1024 sc1 lds\n\t"
    "s_add_u32 m0, s53, 34816\n\t"
    "s_add_u32 s54, s54, 98304\n\t"
    "buffer_load_dwordx4 v1, s[16:19], s54 offen offset:0 sc1 lds\n\t"
    "buffer_load_dwordx4 v1, s[16:19], s54 offen offset:1024 sc1 lds\n\t"
    "s_lshl_b32 s54, s99, 5\n\t"
    "s_add_u32 s54, s54, s65\n\t"
    "s_mul_i32 s54, s54, 20\n\t"
    "s_add_u32 m0, s52, 73728\n\t"
    "s_add_u32 s54, s54, s48\n\t"
    "buffer_load_dwordx4 v8, s[12:15], s54 offen offset:0 lds\n\t"
    "buffer_load_dwordx4 v9, s[12:15], s54 offen offset:1024 lds\n\t"
    "buffer_load_dwordx4 v10, s[12:15], s54 offen offset:2048 lds\n\t"
    "buffer_load_dwordx4 v11, s[12:15], s54 offen offset:3072 lds\n\t"
    "s_waitcnt vmcnt(12)\n\t"
    "s_barrier\n\t"
    "s_lshl_b32 s54, s99, 5\n\t"
    "s_add_u32 s54, s54, s64\n\t"
    "s_add_u32 s54, s54, s50\n\t"
    "s_add_u32 m0, s53, 98304\n\t"
    "s_nop 0\n\t"
    "buffer_load_dwordx4 v1, s[16:19], s54 offen offset:0 sc1 lds\n\t"
    "buffer_load_dwordx4 v1, s[16:19], s54 offen offset:1024 sc1 lds\n\t"
    "s_add_u32 m0, s53, 100352\n\t"
    "s_add_u32 s54, s54, 98304\n\t"
    "buffer_load_dwordx4 v1, s[16:19], s54 offen offset:0 sc1 lds\n\t"
    "buffer_load_dwordx4 v1, s[16:19], s54 offen offset:1024 sc1 lds\n\t"
    "s_lshl_b32 s54, s99, 5\n\t"
    "s_add_u32 s54, s54, s66\n\t"
    "s_mul_i32 s54, s54, 20\n\t"
    "s_add_u32 m0, s52, 0\n\t"
    "s_add_u32 s54, s54, s48\n\t"
    "buffer_load_dwordx4 v8, s[12:15], s54 offen offset:0 lds\n\t"
    "buffer_load_dwordx4 v9, s[12:15], s54 offen offset:1024 lds\n\t"
    "buffer_load_dwordx4 v10, s[12:15], s54 offen offset:2048 lds\n\t"
    "buffer_load_dwordx4 v11, s[12:15], s54 offen offset:3072 lds\n\t"
    "s_waitcnt vmcnt(12)\n\t"
    "s_barrier\n\t"
    "s_lshl_b32 s54, s99, 5\n\t"
    "s_add_u32 s54, s54, s65\n\t"
    "s_add_u32 s54, s54, s50\n\t"
    "s_add_u32 m0, s53, 139264\n\t"
    "s_nop 0\n\t"
    "buffer_load_dwordx4 v1, s[16:19], s54 offen offset:0 sc1 lds\n\t"
    "buffer_load_dwordx4 v1, s[16:19], s54 offen offset:1024 sc1 lds\n\t"
    "s_add_u32 m0, s53, 141312\n\t"
    "s_add_u32 s54, s54, 98304\n\t"
    "buffer_load_dwordx4 v1, s[16:19], s54 offen offset:0 sc1 lds\n\t"
    "buffer_load_dwordx4 v1, s[16:19], s54 offen offset:1024 sc1 lds\n\t"
    "s_lshl_b32 s54, s99, 5\n\t"
    "s_add_u32 s54, s54, s67\n\t"
    "s_mul_i32 s54, s54, 20\n\t"
    "s_add_u32 m0, s52, 57344\n\t"
    "s_add_u32 s54, s54, s48\n\t"
    "buffer_load_dwordx4 v8, s[12:15], s54 offen offset:0 lds\n\t"
    "buffer_load_dwordx4 v9, s[12:15], s54 offen offset:1024 lds\n\t"
    "buffer_load_dwordx4 v10, s[12:15], s54 offen offset:2048 lds\n\t"
    "buffer_load_dwordx4 v11, s[12:15], s54 offen offset:3072 lds\n\t"
    "s_waitcnt vmcnt(12)\n\t"
    "s_barrier\n\t"
    "s_lshl_b32 s54, s99, 5\n\t"
    "s_add_u32 s54, s54, s66\n\t"
    "s_add_u32 s54, s54, s50\n\t"
    "s_add_u32 m0, s53, 32768\n\t"
    "s_nop 0\n\t"
    "buffer_load_dwordx4 v1, s[16:19], s54 offen offset:0 sc1 lds\n\t"
    "buffer_load_dwordx4 v1, s[16:19], s54 offen offset:1024 sc1 lds\n\t"
    "s_add_u32 m0, s53, 34816\n\t"
    "s_add_u32 s54, s54, 98304\n\t"
    "buffer_load_dwordx4 v1, s[16:19], s54 offen offset:0 sc1 lds\n\t"
    "buffer_load_dwordx4 v1, s[16:19], s54 offen offset:1024 sc1 lds\n\t"
    "s_waitcnt vmcnt(8)\n\t"
    "s_barrier\n\t"
    "s_lshl_b32 s54, s99, 5\n\t"
    "s_add_u32 s54, s54, s67\n\t"
    "s_add_u32 s54, s54, s50\n\t"
    "s_add_u32 m0, s53, 90112\n\t"
    "s_nop 0\n\t"
    "buffer_load_dwordx4 v1, s[16:19], s54 offen offset:0 sc1 lds\n\t"
    "buffer_load_dwordx4 v1, s[16:19], s54 offen offset:1024 sc1 lds\n\t"
    "s_add_u32 m0, s53, 92160\n\t"
    "s_add_u32 s54, s54, 98304\n\t"
    "buffer_load_dwordx4 v1, s[16:19], s54 offen offset:0 sc1 lds\n\t"
    "buffer_load_dwordx4 v1, s[16:19], s54 offen offset:1024 sc1 lds\n\t"
    "s_waitcnt vmcnt(4)\n\t"
    "s_barrier\n\t"
    "s_waitcnt vmcnt(0)\n\t"
    "s_barrier\n\t"
    "s_barrier\n\t"
    "s_branch L_end_%=\n\t"
    "L_lspare_%=:\n\t"
    "s_barrier\n\t"
    "s_barrier\n\t"
    "s_barrier\n\t"
    "s_barrier\n\t"
    "s_barrier\n\t"
    "s_branch L_end_%=\n\t"
    "L_compute_%=:\n\t"
    "s_memrealtime s[60:61]\n\t"
    "s_lshl_b32 s44, s44, 1\n\t"
    "s_lshr_b32 s43, s30, 1\n\t"
    "s_add_u32 s44, s44, s43\n\t"
    "s_mul_i32 s44, s44, 96\n\t"
    "s_mul_i32 s45, s45, 12\n\t"
    "s_add_u32 s44, s44, s45\n\t"
    "s_and_b32 s43, s30, 1\n\t"
    "s_lshl_b32 s43, s43, 1\n\t"
    "s_add_u32 s44, s44, s43\n\t"
    "s_lshl_b32 s49, s44, 10\n\t"
    "v_and_b32 v70, 31, v0\n\t"
    "v_lshrrev_b32 v71, 5, v0\n\t"
    "s_lshr_b32 s44, s30, 1\n\t"
    "s_lshl_b32 s44, s44, 5\n\t"
    "v_add_u32 v70, s44, v70\n\t"
    "v_lshrrev_b32 v72, 1, v70\n\t"
    "v_and_b32 v72, 7, v72\n\t"
    "v_lshlrev_b32 v73, 7, v70\n\t"
    "v_or_b32 v74, 0, v71\n\t"
    "v_xor_b32 v74, v74, v72\n\t"
    "v_lshl_add_u32 v2, v74, 4, v73\n\t"
    "v_or_b32 v74, 2, v71\n\t"
    "v_xor_b32 v74, v74, v72\n\t"
    "v_lshl_add_u32 v3, v74, 4, v73\n\t"
    "v_or_b32 v74, 4, v71\n\t"
    "v_xor_b32 v74, v74, v72\n\t"
    "v_lshl_add_u32 v4, v74, 4, v73\n\t"
    "v_or_b32 v74, 6, v71\n\t"
    "v_xor_b32 v74, v74, v72\n\t"
    "v_lshl_add_u32 v5, v74, 4, v73\n\t"
    "v_lshrrev_b32 v74, 4, v0\n\t"
    "v_and_b32 v74, 1, v74\n\t"
    "v_and_b32 v75, 15, v0\n\t"
    "v_lshrrev_b32 v75, 2, v75\n\t"
    "v_and_b32 v76, 3, v0\n\t"
    "v_lshrrev_b32 v77, 1, v76\n\t"
    "v_lshl_add_u32 v77, v74, 1, v77\n\t"
    "v_lshrrev_b32 v78, 1, v75\n\t"
    "v_and_b32 v78, 1, v78\n\t"
    "v_lshlrev_b32 v78, 2, v78\n\t"
    "v_and_b32 v76, 1, v76\n\t"
    "v_lshlrev_b32 v76, 3, v76\n\t"
    "s_and_b32 s44, s30, 1\n\t"
    "s_lshl_b32 s44, s44, 2\n\t"
    "v_or_b32 v79, s44, v77\n\t"
    "v_xor_b32 v79, v79, v78\n\t"
    "v_lshl_add_u32 v79, v79, 4, v76\n\t"
    "v_lshl_add_u32 v82, v71, 3, v75\n\t"
    "v_lshl_add_u32 v6, v82, 7, v79\n\t"
    "v_lshl_add_u32 v82, v71, 2, v75\n\t"
    "v_lshl_add_u32 v9, v82, 7, v79\n\t"
    "s_waitcnt lgkmcnt(0)\n\t"
    "s_add_u32 s24, s72, 8388608\n\t"
    "s_addc_u32 s25, s73, 0\n\t"
    "s_and_b32 s25, s25, 0xffff\n\t"
    "s_mov_b32 s26, 262144\n\t"
    "s_mov_b32 s27, 0x20000\n\t"
    "s_lshl_b32 s28, s81, 5\n\t"
    "s_add_u32 s28, s28, s41\n\t"
    "s_lshl_b32 s57, s80, 16\n\t"
    "s_cmp_lg_u32 s40, 0\n\t"
    "s_cbranch_scc1 L_ce1_%=\n\t"
    "s_lshl_b64 s[60:61], s[60:61], 2\n\t"
    "s_lshl_b32 s45, s28, 3\n\t"
    "v_mov_b32 v89, s45\n\t"
    "v_mov_b32 v84, s60\n\t"
    "v_mov_b32 v85, s61\n\t"
    "buffer_store_dwordx2 v[84:85], v89, s[24:27], s57 offen sc1\n\t"
    "L_ce1_%=:\n\t"
    "s_mov_b32 s16, s72\n\t"
    "s_and_b32 s17, s73, 0xffff\n\t"
    "s_mov_b32 s18, 6291456\n\t"
    "s_mov_b32 s19, 0x20000\n\t"
    "s_mov_b32 s20, s70\n\t"
    "s_and_b32 s21, s71, 0xffff\n\t"
    "s_mov_b32 s22, 2621440\n\t"
    "s_mov_b32 s23, 0x20000\n\t"
    "s_mul_i32 s44, s41, 205\n\t"
    "s_lshr_b32 s44, s44, 10\n\t"
    "s_mul_i32 s45, s44, 5\n\t"
    "s_sub_u32 s45, s41, s45\n\t"
    "s_add_u32 s31, s44, s45\n\t"
    "s_and_b32 s31, s31, 3\n\t"
    "s_mul_i32 s43, s81, 5\n\t"
    "s_add_u32 s45, s45, s43\n\t"
    "s_mov_b32 s56, 3\n\t"
    "s_cmp_ge_u32 s44, 4\n\t"
    "s_cselect_b32 s56, 2, s56\n\t"
    "s_cmp_ge_u32 s44, 6\n\t"
    "s_cselect_b32 s56, 0, s56\n\t"
    "s_mul_i32 s43, s44, 3\n\t"
    "s_lshl_b32 s55, s44, 1\n\t"
    "s_add_u32 s55, s55, 4\n\t"
    "s_min_u32 s43, s43, s55\n\t"
    "s_lshl_b32 s55, s80, 4\n\t"
    "s_add_u32 s43, s43, s55\n\t"
    "s_mov_b32 s58, 0\n\t"
    "s_cmp_eq_u32 s44, 0\n\t"
    "s_cselect_b32 s58, 0xff, s58\n\t"
    "s_cmp_eq_u32 s44, 1\n\t"
    "s_cselect_b32 s58, 0xff0, s58\n\t"
    "s_cmp_eq_u32 s44, 2\n\t"
    "s_cselect_b32 s58, 0xff000, s58\n\t"
    "s_cmp_eq_u32 s44, 3\n\t"
    "s_cselect_b32 s58, 0xff0000, s58\n\t"
    "s_cmp_eq_u32 s44, 4\n\t"
    "s_cselect_b32 s58, 0xf000000, s58\n\t"
    "s_cmp_eq_u32 s44, 5\n\t"
    "s_cselect_b32 s58, 0xf0000000, s58\n\t"
    "s_mov_b32 s59, s58\n\t"
    "s_mul_i32 s51, s43, 40960\n\t"
    "s_lshl_b32 s55, s45, 7\n\t"
    "s_add_u32 s51, s51, s55\n\t"
    "s_lshr_b32 s43, s30, 1\n\t"
    "s_lshl_b32 s68, s43, 1\n\t"
    "s_add_u32 s69, s43, 1\n\t"
    "s_xor_b32 s70, s43, 1\n\t"
    "s_cmp_eq_u32 s56, 3\n\t"
    "s_cselect_b32 s68, s68, s43\n\t"
    "s_cselect_b32 s69, s69, s43\n\t"
    "s_cselect_b32 s70, s70, 0\n\t"
    "s_lshl_b32 s44, s68, 11\n\t"
    "v_add_u32 v10, s44, v1\n\t"
    "s_lshl_b32 s44, s69, 11\n\t"
    "v_add_u32 v11, s44, v1\n\t"
    "v_and_b32 v70, 31, v0\n\t"
    "s_movk_i32 s44, 144\n\t"
    "v_mul_lo_u32 v72, v70, s44\n\t"
    "v_lshl_add_u32 v72, v71, 3, v72\n\t"
    "s_and_b32 s43, s30, 1\n\t"
    "s_lshl_b32 s43, s43, 6\n\t"
    "v_add_u32 v15, s43, v72\n\t"
    "v_lshrrev_b32 v70, 3, v0\n\t"
    "v_and_b32 v71, 7, v0\n\t"
    "v_mul_lo_u32 v72, v70, s44\n\t"
    "v_lshl_add_u32 v80, v71, 4, v72\n\t"
    "s_movk_i32 s44, 1280\n\t"
    "v_mul_lo_u32 v72, v70, s44\n\t"
    "v_lshl_add_u32 v81, v71, 4, v72\n\t"
    "s_barrier\n\t"
    "v_add_u32 v7, 49152, v6\n\t"
    "ds_read_b64_tr_b16 v[48:49], v7 offset:0\n\t"
    "ds_read_b64_tr_b16 v[50:51], v7 offset:512\n\t"
    "ds_read_b128 a[0:3], v2 offset:0\n\t"
    "ds_read_b64_tr_b16 v[52:53], v7 offset:2048\n\t"
    "ds_read_b64_tr_b16 v[54:55], v7 offset:2560\n\t"
    "ds_read_b128 a[4:7], v3 offset:0\n\t"
    "ds_read_b64_tr_b16 v[56:57], v7 offset:4096\n\t"
    "ds_read_b64_tr_b16 v[58:59], v7 offset:4608\n\t"
    "ds_read_b128 a[8:11], v4 offset:0\n\t"
    "ds_read_b64_tr_b16 v[60:61], v7 offset:6144\n\t"
    "ds_read_b64_tr_b16 v[62:63], v7 offset:6656\n\t"
    "ds_read_b128 a[12:15], v5 offset:0\n\t"
    "ds_read_b64_tr_b16 v[64:65], v7 offset:8192\n\t"
    "ds_read_b64_tr_b16 v[66:67], v7 offset:8704\n\t"
    "ds_read_b128 a[16:19], v2 offset:8192\n\t"
    "s_waitcnt lgkmcnt(12)\n\t"
    "v_mfma_f32_32x32x16_bf16 v[16:31], v[48:51], a[0:3], 0\n\t"
    "ds_read_b64_tr_b16 v[68:69], v7 offset:10240\n\t"
    "ds_read_b64_tr_b16 v[70:71], v7 offset:10752\n\t"
    "ds_read_b128 a[20:23], v3 offset:8192\n\t"
    "s_waitcnt lgkmcnt(12)\n\t"
    "v_mfma_f32_32x32x16_bf16 v[16:31], v[52:55], a[4:7], v[16:31]\n\t"
    "ds_read_b64_tr_b16 v[72:73], v7 offset:12288\n\t"
    "ds_read_b64_tr_b16 v[74:75], v7 offset:12800\n\t"
    "ds_read_b128 a[24:27], v4 offset:8192\n\t"
    "s_waitcnt lgkmcnt(12)\n\t"
    "v_mfma_f32_32x32x16_bf16 v[16:31], v[56:59], a[8:11], v[16:31]\n\t"
    "ds_read_b64_tr_b16 v[76:77], v7 offset:14336\n\t"
    "ds_read_b64_tr_b16 v[78:79], v7 offset:14848\n\t"
    "ds_read_b128 a[28:31], v5 offset:8192\n\t"
    "s_waitcnt lgkmcnt(12)\n\t"
    "v_mfma_f32_32x32x16_bf16 v[16:31], v[60:63], a[12:15], v[16:31]\n\t"
    "s_waitcnt lgkmcnt(9)\n\t"
    "v_mfma_f32_32x32x16_bf16 v[16:31], v[64:67], a[16:19], v[16:31]\n\t"
    "s_waitcnt lgkmcnt(0)\n\t"
    "s_barrier\n\t"
    "v_add_u32 v8, 147456, v6\n\t"
    "ds_read_b64_tr_b16 v[48:49], v8 offset:0\n\t"
    "ds_read_b64_tr_b16 v[50:51], v8 offset:512\n\t"
    "ds_read_b128 a[32:35], v2 offset:16384\n\t"
    "ds_read_b64_tr_b16 v[52:53], v8 offset:2048\n\t"
    "ds_read_b64_tr_b16 v[54:55], v8 offset:2560\n\t"
    "ds_read_b128 a[36:39], v3 offset:16384\n\t"
    "ds_read_b64_tr_b16 v[56:57], v8 offset:4096\n\t"
    "ds_read_b64_tr_b16 v[58:59], v8 offset:4608\n\t"
    "ds_read_b128 a[40:43], v4 offset:16384\n\t"
    "ds_read_b64_tr_b16 v[60:61], v8 offset:6144\n\t"
    "ds_read_b64_tr_b16 v[62:63], v8 offset:6656\n\t"
    "ds_read_b128 a[44:47], v5 offset:16384\n\t"
    "v_mfma_f32_32x32x16_bf16 v[16:31], v[68:71], a[20:23], v[16:31]\n\t"
    "ds_read_b64_tr_b16 v[64:65], v8 offset:8192\n\t"
    "ds_read_b64_tr_b16 v[66:67], v8 offset:8704\n\t"
    "ds_read_b128 a[48:51], v2 offset:24576\n\t"
    "v_mfma_f32_32x32x16_bf16 v[16:31], v[72:75], a[24:27], v[16:31]\n\t"
    "ds_read_b64_tr_b16 v[68:69], v8 offset:10240\n\t"
    "ds_read_b64_tr_b16 v[70:71], v8 offset:10752\n\t"
    "ds_read_b128 a[52:55], v3 offset:24576\n\t"
    "v_mfma_f32_32x32x16_bf16 v[16:31], v[76:79], a[28:31], v[16:31]\n\t"
    "ds_read_b64_tr_b16 v[72:73], v8 offset:12288\n\t"
    "ds_read_b64_tr_b16 v[74:75], v8 offset:12800\n\t"
    "ds_read_b128 a[56:59], v4 offset:24576\n\t"
    "s_waitcnt lgkmcnt(15)\n\t"
    "v_mfma_f32_32x32x16_bf16 v[16:31], v[48:51], a[32:35], v[16:31]\n\t"
    "ds_read_b64_tr_b16 v[76:77], v8 offset:14336\n\t"
    "ds_read_b64_tr_b16 v[78:79], v8 offset:14848\n\t"
    "ds_read_b128 a[60:63], v5 offset:24576\n\t"
    "v_mfma_f32_32x32x16_bf16 v[16:31], v[52:55], a[36:39], v[16:31]\n\t"
    "s_waitcnt lgkmcnt(15)\n\t"
    "v_mfma_f32_32x32x16_bf16 v[16:31], v[56:59], a[40:43], v[16:31]\n\t"
    "s_waitcnt lgkmcnt(12)\n\t"
    "v_mfma_f32_32x32x16_bf16 v[16:31], v[60:63], a[44:47], v[16:31]\n\t"
    "s_waitcnt lgkmcnt(9)\n\t"
    "v_mfma_f32_32x32x16_bf16 v[16:31], v[64:67], a[48:51], v[16:31]\n\t"
    "s_waitcnt lgkmcnt(0)\n\t"
    "s_barrier\n\t"
    "v_add_u32 v7, 131072, v6\n\t"
    "ds_read_b64_tr_b16 v[48:49], v7 offset:0\n\t"
    "ds_read_b64_tr_b16 v[50:51], v7 offset:512\n\t"
    "ds_read_b128 a[64:67], v2 offset:32768\n\t"
    "ds_read_b64_tr_b16 v[52:53], v7 offset:2048\n\t"
    "ds_read_b64_tr_b16 v[54:55], v7 offset:2560\n\t"
    "ds_read_b128 a[68:71], v3 offset:32768\n\t"
    "ds_read_b64_tr_b16 v[56:57], v7 offset:4096\n\t"
    "ds_read_b64_tr_b16 v[58:59], v7 offset:4608\n\t"
    "ds_read_b128 a[72:75], v4 offset:32768\n\t"
    "ds_read_b64_tr_b16 v[60:61], v7 offset:6144\n\t"
    "ds_read_b64_tr_b16 v[62:63], v7 offset:6656\n\t"
    "ds_read_b128 a[76:79], v5 offset:32768\n\t"
    "v_mfma_f32_32x32x16_bf16 v[16:31], v[68:71], a[52:55], v[16:31]\n\t"
    "ds_read_b64_tr_b16 v[64:65], v7 offset:8192\n\t"
    "ds_read_b64_tr_b16 v[66:67], v7 offset:8704\n\t"
    "ds_read_b128 a[80:83], v2 offset:40960\n\t"
    "v_mfma_f32_32x32x16_bf16 v[16:31], v[72:75], a[56:59], v[16:31]\n\t"
    "ds_read_b64_tr_b16 v[68:69], v7 offset:10240\n\t"
    "ds_read_b64_tr_b16 v[70:71], v7 offset:10752\n\t"
    "ds_read_b128 a[84:87], v3 offset:40960\n\t"
    "v_mfma_f32_32x32x16_bf16 v[16:31], v[76:79], a[60:63], v[16:31]\n\t"
    "ds_read_b64_tr_b16 v[72:73], v7 offset:12288\n\t"
    "ds_read_b64_tr_b16 v[74:75], v7 offset:12800\n\t"
    "ds_read_b128 a[88:91], v4 offset:40960\n\t"
    "s_waitcnt lgkmcnt(15)\n\t"
    "v_mfma_f32_32x32x16_bf16 v[16:31], v[48:51], a[64:67], v[16:31]\n\t"
    "ds_read_b64_tr_b16 v[76:77], v7 offset:14336\n\t"
    "ds_read_b64_tr_b16 v[78:79], v7 offset:14848\n\t"
    "ds_read_b128 a[92:95], v5 offset:40960\n\t"
    "v_mfma_f32_32x32x16_bf16 v[16:31], v[52:55], a[68:71], v[16:31]\n\t"
    "s_waitcnt lgkmcnt(15)\n\t"
    "v_mfma_f32_32x32x16_bf16 v[16:31], v[56:59], a[72:75], v[16:31]\n\t"
    "s_waitcnt lgkmcnt(12)\n\t"
    "v_mfma_f32_32x32x16_bf16 v[16:31], v[60:63], a[76:79], v[16:31]\n\t"
    "s_waitcnt lgkmcnt(9)\n\t"
    "v_mfma_f32_32x32x16_bf16 v[16:31], v[64:67], a[80:83], v[16:31]\n\t"
    "s_waitcnt lgkmcnt(0)\n\t"
    "s_barrier\n\t"
    "v_add_u32 v8, 0, v6\n\t"
    "ds_read_b64_tr_b16 v[48:49], v8 offset:0\n\t"
    "ds_read_b64_tr_b16 v[50:51], v8 offset:512\n\t"
    "ds_read_b128 a[96:99], v2 offset:49152\n\t"
    "ds_read_b64_tr_b16 v[52:53], v8 offset:2048\n\t"
    "ds_read_b64_tr_b16 v[54:55], v8 offset:2560\n\t"
    "ds_read_b128 a[100:103], v3 offset:49152\n\t"
    "ds_read_b64_tr_b16 v[56:57], v8 offset:4096\n\t"
    "ds_read_b64_tr_b16 v[58:59], v8 offset:4608\n\t"
    "ds_read_b128 a[104:107], v4 offset:49152\n\t"
    "ds_read_b64_tr_b16 v[60:61], v8 offset:6144\n\t"
    "ds_read_b64_tr_b16 v[62:63], v8 offset:6656\n\t"
    "ds_read_b128 a[108:111], v5 offset:49152\n\t"
    "v_mfma_f32_32x32x16_bf16 v[16:31], v[68:71], a[84:87], v[16:31]\n\t"
    "ds_read_b64_tr_b16 v[64:65], v8 offset:8192\n\t"
    "ds_read_b64_tr_b16 v[66:67], v8 offset:8704\n\t"
    "ds_read_b128 a[112:115], v2 offset:57344\n\t"
    "v_mfma_f32_32x32x16_bf16 v[16:31], v[72:75], a[88:91], v[16:31]\n\t"
    "ds_read_b64_tr_b16 v[68:69], v8 offset:10240\n\t"
    "ds_read_b64_tr_b16 v[70:71], v8 offset:10752\n\t"
    "ds_read_b128 a[116:119], v3 offset:57344\n\t"
    "v_mfma_f32_32x32x16_bf16 v[16:31], v[76:79], a[92:95], v[16:31]\n\t"
    "ds_read_b64_tr_b16 v[72:73], v8 offset:12288\n\t"
    "ds_read_b64_tr_b16 v[74:75], v8 offset:12800\n\t"
    "ds_read_b128 a[120:123], v4 offset:57344\n\t"
    "s_waitcnt lgkmcnt(15)\n\t"
    "v_mfma_f32_32x32x16_bf16 v[16:31], v[48:51], a[96:99], v[16:31]\n\t"
    "ds_read_b64_tr_b16 v[76:77], v8 offset:14336\n\t"
    "ds_read_b64_tr_b16 v[78:79], v8 offset:14848\n\t"
    "ds_read_b128 a[124:127], v5 offset:57344\n\t"
    "v_mfma_f32_32x32x16_bf16 v[16:31], v[52:55], a[100:103], v[16:31]\n\t"
    "s_waitcnt lgkmcnt(15)\n\t"
    "v_mfma_f32_32x32x16_bf16 v[16:31], v[56:59], a[104:107], v[16:31]\n\t"
    "s_waitcnt lgkmcnt(12)\n\t"
    "v_mfma_f32_32x32x16_bf16 v[16:31], v[60:63], a[108:111], v[16:31]\n\t"
    "s_waitcnt lgkmcnt(9)\n\t"
    "v_mfma_f32_32x32x16_bf16 v[16:31], v[64:67], a[112:115], v[16:31]\n\t"
    "s_waitcnt lgkmcnt(0)\n\t"
    "s_barrier\n\t"
    "v_add_u32 v7, 81920, v6\n\t"
    "ds_read_b64_tr_b16 v[48:49], v7 offset:0\n\t"
    "ds_read_b64_tr_b16 v[50:51], v7 offset:512\n\t"
    "v_add_u32 v2, 0x10000, v2\n\t"
    "v_add_u32 v3, 0x10000, v3\n\t"
    "v_add_u32 v4, 0x10000, v4\n\t"
    "v_add_u32 v5, 0x10000, v5\n\t"
    "ds_read_b128 a[128:131], v2 offset:0\n\t"
    "ds_read_b64_tr_b16 v[52:53], v7 offset:2048\n\t"
    "ds_read_b64_tr_b16 v[54:55], v7 offset:2560\n\t"
    "ds_read_b128 a[132:135], v3 offset:0\n\t"
    "ds_read_b64_tr_b16 v[56:57], v7 offset:4096\n\t"
    "ds_read_b64_tr_b16 v[58:59], v7 offset:4608\n\t"
    "ds_read_b128 a[136:139], v4 offset:0\n\t"
    "ds_read_b64_tr_b16 v[60:61], v7 offset:6144\n\t"
    "ds_read_b64_tr_b16 v[62:63], v7 offset:6656\n\t"
    "ds_read_b128 a[140:143], v5 offset:0\n\t"
    "v_mfma_f32_32x32x16_bf16 v[16:31], v[68:71], a[116:119], v[16:31]\n\t"
    "ds_read_b64_tr_b16 v[64:65], v7 offset:8192\n\t"
    "ds_read_b64_tr_b16 v[66:67], v7 offset:8704\n\t"
    "ds_read_b128 a[144:147], v2 offset:8192\n\t"
    "v_mfma_f32_32x32x16_bf16 v[16:31], v[72:75], a[120:123], v[16:31]\n\t"
    "ds_read_b64_tr_b16 v[68:69], v7 offset:10240\n\t"
    "ds_read_b64_tr_b16 v[70:71], v7 offset:10752\n\t"
    "ds_read_b128 a[148:151], v3 offset:8192\n\t"
    "v_mfma_f32_32x32x16_bf16 v[16:31], v[76:79], a[124:127], v[16:31]\n\t"
    "ds_read_b64_tr_b16 v[72:73], v7 offset:12288\n\t"
    "ds_read_b64_tr_b16 v[74:75], v7 offset:12800\n\t"
    "ds_read_b128 a[152:155], v4 offset:8192\n\t"
    "s_waitcnt lgkmcnt(15)\n\t"
    "v_mfma_f32_32x32x16_bf16 v[16:31], v[48:51], a[128:131], v[16:31]\n\t"
    "ds_read_b64_tr_b16 v[76:77], v7 offset:14336\n\t"
    "ds_read_b64_tr_b16 v[78:79], v7 offset:14848\n\t"
    "ds_read_b128 a[156:159], v5 offset:8192\n\t"
    "v_mfma_f32_32x32x16_bf16 v[16:31], v[52:55], a[132:135], v[16:31]\n\t"
    "s_waitcnt lgkmcnt(15)\n\t"
    "v_mfma_f32_32x32x16_bf16 v[16:31], v[56:59], a[136:139], v[16:31]\n\t"
    "s_waitcnt lgkmcnt(12)\n\t"
    "v_mfma_f32_32x32x16_bf16 v[16:31], v[60:63], a[140:143], v[16:31]\n\t"
    "s_waitcnt lgkmcnt(9)\n\t"
    "v_mfma_f32_32x32x16_bf16 v[16:31], v[64:67], a[144:147], v[16:31]\n\t"
    "s_waitcnt lgkmcnt(6)\n\t"
    "v_mfma_f32_32x32x16_bf16 v[16:31], v[68:71], a[148:151], v[16:31]\n\t"
    "s_waitcnt lgkmcnt(3)\n\t"
    "v_mfma_f32_32x32x16_bf16 v[16:31], v[72:75], a[152:155], v[16:31]\n\t"
    "s_waitcnt lgkmcnt(0)\n\t"
    "v_mfma_f32_32x32x16_bf16 v[16:31], v[76:79], a[156:159], v[16:31]\n\t"
    "s_nop 12\n\t"
    "v_maximum3_f32 v16, v16, 0, 0\n\t"
    "v_maximum3_f32 v17, v17, 0, 0\n\t"
    "v_maximum3_f32 v18, v18, 0, 0\n\t"
    "v_maximum3_f32 v19, v19, 0, 0\n\t"
    "v_maximum3_f32 v20, v20, 0, 0\n\t"
    "v_maximum3_f32 v21, v21, 0, 0\n\t"
    "v_maximum3_f32 v22, v22, 0, 0\n\t"
    "v_maximum3_f32 v23, v23, 0, 0\n\t"
    "v_maximum3_f32 v24, v24, 0, 0\n\t"
    "v_maximum3_f32 v25, v25, 0, 0\n\t"
    "v_maximum3_f32 v26, v26, 0, 0\n\t"
    "v_maximum3_f32 v27, v27, 0, 0\n\t"
    "v_maximum3_f32 v28, v28, 0, 0\n\t"
    "v_maximum3_f32 v29, v29, 0, 0\n\t"
    "v_maximum3_f32 v30, v30, 0, 0\n\t"
    "v_maximum3_f32 v31, v31, 0, 0\n\t"
    "v_cvt_pk_bf16_f32 v84, v16, v17\n\t"
    "v_cvt_pk_bf16_f32 v85, v18, v19\n\t"
    "v_cvt_pk_bf16_f32 v86, v20, v21\n\t"
    "v_cvt_pk_bf16_f32 v87, v22, v23\n\t"
    "v_cvt_pk_bf16_f32 v88, v24, v25\n\t"
    "v_cvt_pk_bf16_f32 v89, v26, v27\n\t"
    "v_cvt_pk_bf16_f32 v90, v28, v29\n\t"
    "v_cvt_pk_bf16_f32 v91, v30, v31\n\t"
    "s_lshl_b32 s55, s97, 5\n\t"
    "s_add_u32 s55, s55, s49\n\t"
    "buffer_store_dwordx4 v[84:87], v1, s[16:19], s55 offen sc1\n\t"
    "buffer_store_dwordx4 v[88:91], v1, s[16:19], s55 offen offset:1024 sc1\n\t"
    "s_barrier\n\t"
    "s_cmp_lg_u32 s40, 0\n\t"
    "s_cbranch_scc1 L_pr_%=\n\t"
    "v_lshlrev_b32 v82, 3, v0\n\t"
    "buffer_load_dwordx2 v[92:93], v82, s[24:27], s57 offen sc1\n\t"
    "L_pr_%=:\n\t"
    "v_add_u32 v8, 131072, v6\n\t"
    "ds_read_b64_tr_b16 v[48:49], v8 offset:0\n\t"
    "ds_read_b64_tr_b16 v[50:51], v8 offset:512\n\t"
    "ds_read_b64_tr_b16 v[52:53], v8 offset:2048\n\t"
    "ds_read_b64_tr_b16 v[54:55], v8 offset:2560\n\t"
    "ds_read_b64_tr_b16 v[56:57], v8 offset:4096\n\t"
    "ds_read_b64_tr_b16 v[58:59], v8 offset:4608\n\t"
    "ds_read_b64_tr_b16 v[60:61], v8 offset:6144\n\t"
    "ds_read_b64_tr_b16 v[62:63], v8 offset:6656\n\t"
    "ds_read_b64_tr_b16 v[64:65], v8 offset:8192\n\t"
    "ds_read_b64_tr_b16 v[66:67], v8 offset:8704\n\t"
    "s_waitcnt lgkmcnt(8)\n\t"
    "v_mfma_f32_32x32x16_bf16 v[32:47], v[48:51], a[0:3], 0\n\t"
    "ds_read_b64_tr_b16 v[68:69], v8 offset:10240\n\t"
    "ds_read_b64_tr_b16 v[70:71], v8 offset:10752\n\t"
    "s_waitcnt lgkmcnt(8)\n\t"
    "v_mfma_f32_32x32x16_bf16 v[32:47], v[52:55], a[4:7], v[32:47]\n\t"
    "ds_read_b64_tr_b16 v[72:73], v8 offset:12288\n\t"
    "ds_read_b64_tr_b16 v[74:75], v8 offset:12800\n\t"
    "s_waitcnt lgkmcnt(8)\n\t"
    "v_mfma_f32_32x32x16_bf16 v[32:47], v[56:59], a[8:11], v[32:47]\n\t"
    "ds_read_b64_tr_b16 v[76:77], v8 offset:14336\n\t"
    "ds_read_b64_tr_b16 v[78:79], v8 offset:14848\n\t"
    "s_waitcnt lgkmcnt(8)\n\t"
    "v_mfma_f32_32x32x16_bf16 v[32:47], v[60:63], a[12:15], v[32:47]\n\t"
    "v_add_u32 v7, 147456, v6\n\t"
    "ds_read_b64_tr_b16 v[48:49], v7 offset:0\n\t"
    "ds_read_b64_tr_b16 v[50:51], v7 offset:512\n\t"
    "s_waitcnt lgkmcnt(8)\n\t"
    "v_mfma_f32_32x32x16_bf16 v[32:47], v[64:67], a[16:19], v[32:47]\n\t"
    "ds_read_b64_tr_b16 v[52:53], v7 offset:2048\n\t"
    "ds_read_b64_tr_b16 v[54:55], v7 offset:2560\n\t"
    "s_waitcnt lgkmcnt(8)\n\t"
    "v_mfma_f32_32x32x16_bf16 v[32:47], v[68:71], a[20:23], v[32:47]\n\t"
    "ds_read_b64_tr_b16 v[56:57], v7 offset:4096\n\t"
    "ds_read_b64_tr_b16 v[58:59], v7 offset:4608\n\t"
    "s_waitcnt lgkmcnt(8)\n\t"
    "v_mfma_f32_32x32x16_bf16 v[32:47], v[72:75], a[24:27], v[32:47]\n\t"
    "ds_read_b64_tr_b16 v[60:61], v7 offset:6144\n\t"
    "ds_read_b64_tr_b16 v[62:63], v7 offset:6656\n\t"
    "s_waitcnt lgkmcnt(8)\n\t"
    "v_mfma_f32_32x32x16_bf16 v[32:47], v[76:79], a[28:31], v[32:47]\n\t"
    "ds_read_b64_tr_b16 v[64:65], v7 offset:8192\n\t"
    "ds_read_b64_tr_b16 v[66:67], v7 offset:8704\n\t"
    "s_waitcnt lgkmcnt(8)\n\t"
    "v_mfma_f32_32x32x16_bf16 v[16:31], v[48:51], a[0:3], 0\n\t"
    "ds_read_b64_tr_b16 v[68:69], v7 offset:10240\n\t"
    "ds_read_b64_tr_b16 v[70:71], v7 offset:10752\n\t"
    "s_waitcnt lgkmcnt(8)\n\t"
    "v_mfma_f32_32x32x16_bf16 v[16:31], v[52:55], a[4:7], v[16:31]\n\t"
    "ds_read_b64_tr_b16 v[72:73], v7 offset:12288\n\t"
    "ds_read_b64_tr_b16 v[74:75], v7 offset:12800\n\t"
    "s_waitcnt lgkmcnt(8)\n\t"
    "v_mfma_f32_32x32x16_bf16 v[16:31], v[56:59], a[8:11], v[16:31]\n\t"
    "ds_read_b64_tr_b16 v[76:77], v7 offset:14336\n\t"
    "ds_read_b64_tr_b16 v[78:79], v7 offset:14848\n\t"
    "s_waitcnt lgkmcnt(8)\n\t"
    "v_mfma_f32_32x32x16_bf16 v[16:31], v[60:63], a[12:15], v[16:31]\n\t"
    "s_waitcnt lgkmcnt(6)\n\t"
    "v_mfma_f32_32x32x16_bf16 v[16:31], v[64:67], a[16:19], v[16:31]\n\t"
    "s_waitcnt lgkmcnt(0)\n\t"
    "s_barrier\n\t"
    "v_add_u32 v7, 98304, v6\n\t"
    "ds_read_b64_tr_b16 v[48:49], v7 offset:0\n\t"
    "ds_read_b64_tr_b16 v[50:51], v7 offset:512\n\t"
    "ds_read_b64_tr_b16 v[52:53], v7 offset:2048\n\t"
    "ds_read_b64_tr_b16 v[54:55], v7 offset:2560\n\t"
    "ds_read_b64_tr_b16 v[56:57], v7 offset:4096\n\t"
    "ds_read_b64_tr_b16 v[58:59], v7 offset:4608\n\t"
    "ds_read_b64_tr_b16 v[60:61], v7 offset:6144\n\t"
    "ds_read_b64_tr_b16 v[62:63], v7 offset:6656\n\t"
    "v_mfma_f32_32x32x16_bf16 v[16:31], v[68:71], a[20:23], v[16:31]\n\t"
    "ds_read_b64_tr_b16 v[64:65], v7 offset:8192\n\t"
    "ds_read_b64_tr_b16 v[66:67], v7 offset:8704\n\t"
    "v_mfma_f32_32x32x16_bf16 v[16:31], v[72:75], a[24:27], v[16:31]\n\t"
    "ds_read_b64_tr_b16 v[68:69], v7 offset:10240\n\t"
    "ds_read_b64_tr_b16 v[70:71], v7 offset:10752\n\t"
    "v_mfma_f32_32x32x16_bf16 v[16:31], v[76:79], a[28:31], v[16:31]\n\t"
    "ds_read_b64_tr_b16 v[72:73], v7 offset:12288\n\t"
    "ds_read_b64_tr_b16 v[74:75], v7 offset:12800\n\t"
    "s_waitcnt lgkmcnt(12)\n\t"
    "v_mfma_f32_32x32x16_bf16 v[32:47], v[48:51], a[32:35], v[32:47]\n\t"
    "ds_read_b64_tr_b16 v[76:77], v7 offset:14336\n\t"
    "ds_read_b64_tr_b16 v[78:79], v7 offset:14848\n\t"
    "s_waitcnt lgkmcnt(12)\n\t"
    "v_mfma_f32_32x32x16_bf16 v[32:47], v[52:55], a[36:39], v[32:47]\n\t"
    "v_add_u32 v8, 114688, v6\n\t"
    "ds_read_b64_tr_b16 v[48:49], v8 offset:0\n\t"
    "ds_read_b64_tr_b16 v[50:51], v8 offset:512\n\t"
    "s_waitcnt lgkmcnt(12)\n\t"
    "v_mfma_f32_32x32x16_bf16 v[32:47], v[56:59], a[40:43], v[32:47]\n\t"
    "ds_read_b64_tr_b16 v[52:53], v8 offset:2048\n\t"
    "ds_read_b64_tr_b16 v[54:55], v8 offset:2560\n\t"
    "s_waitcnt lgkmcnt(12)\n\t"
    "v_mfma_f32_32x32x16_bf16 v[32:47], v[60:63], a[44:47], v[32:47]\n\t"
    "ds_read_b64_tr_b16 v[56:57], v8 offset:4096\n\t"
    "ds_read_b64_tr_b16 v[58:59], v8 offset:4608\n\t"
    "s_waitcnt lgkmcnt(12)\n\t"
    "v_mfma_f32_32x32x16_bf16 v[32:47], v[64:67], a[48:51], v[32:47]\n\t"
    "ds_read_b64_tr_b16 v[60:61], v8 offset:6144\n\t"
    "ds_read_b64_tr_b16 v[62:63], v8 offset:6656\n\t"
    "s_waitcnt lgkmcnt(12)\n\t"
    "v_mfma_f32_32x32x16_bf16 v[32:47], v[68:71], a[52:55], v[32:47]\n\t"
    "ds_read_b64_tr_b16 v[64:65], v8 offset:8192\n\t"
    "ds_read_b64_tr_b16 v[66:67], v8 offset:8704\n\t"
    "s_waitcnt lgkmcnt(12)\n\t"
    "v_mfma_f32_32x32x16_bf16 v[32:47], v[72:75], a[56:59], v[32:47]\n\t"
    "ds_read_b64_tr_b16 v[68:69], v8 offset:10240\n\t"
    "ds_read_b64_tr_b16 v[70:71], v8 offset:10752\n\t"
    "s_waitcnt lgkmcnt(12)\n\t"
    "v_mfma_f32_32x32x16_bf16 v[32:47], v[76:79], a[60:63], v[32:47]\n\t"
    "ds_read_b64_tr_b16 v[72:73], v8 offset:12288\n\t"
    "ds_read_b64_tr_b16 v[74:75], v8 offset:12800\n\t"
    "s_waitcnt lgkmcnt(12)\n\t"
    "v_mfma_f32_32x32x16_bf16 v[32:47], v[48:51], a[64:67], v[32:47]\n\t"
    "ds_read_b64_tr_b16 v[76:77], v8 offset:14336\n\t"
    "ds_read_b64_tr_b16 v[78:79], v8 offset:14848\n\t"
    "s_waitcnt lgkmcnt(12)\n\t"
    "v_mfma_f32_32x32x16_bf16 v[32:47], v[52:55], a[68:71], v[32:47]\n\t"
    "s_waitcnt lgkmcnt(10)\n\t"
    "v_mfma_f32_32x32x16_bf16 v[32:47], v[56:59], a[72:75], v[32:47]\n\t"
    "s_waitcnt lgkmcnt(8)\n\t"
    "v_mfma_f32_32x32x16_bf16 v[32:47], v[60:63], a[76:79], v[32:47]\n\t"
    "s_waitcnt lgkmcnt(6)\n\t"
    "v_mfma_f32_32x32x16_bf16 v[32:47], v[64:67], a[80:83], v[32:47]\n\t"
    "s_waitcnt vmcnt(0)\n\t"
    "s_waitcnt lgkmcnt(0)\n\t"
    "s_barrier\n\t"
    "s_cmp_lg_u32 s40, 0\n\t"
    "s_cbranch_scc1 L_ae1_%=\n\t"
    "s_waitcnt vmcnt(0)\n\t"
    "v_and_b32 v92, -4, v92\n\t"
    "v_add_u32 v86, 1, v92\n\t"
    "v_mov_b32 v87, v93\n\t"
    "s_lshl_b32 s44, s28, 9\n\t"
    "s_add_u32 s44, s44, 4096\n\t"
    "v_lshl_add_u32 v83, v0, 3, s44\n\t"
    "buffer_store_dwordx2 v[86:87], v83, s[24:27], s57 offen sc1\n\t"
    "L_ae1_%=:\n\t"
    "v_add_u32 v7, 8192, v6\n\t"
    "ds_read_b64_tr_b16 v[48:49], v7 offset:0\n\t"
    "ds_read_b64_tr_b16 v[50:51], v7 offset:512\n\t"
    "ds_read_b64_tr_b16 v[52:53], v7 offset:2048\n\t"
    "ds_read_b64_tr_b16 v[54:55], v7 offset:2560\n\t"
    "ds_read_b64_tr_b16 v[56:57], v7 offset:4096\n\t"
    "ds_read_b64_tr_b16 v[58:59], v7 offset:4608\n\t"
    "ds_read_b64_tr_b16 v[60:61], v7 offset:6144\n\t"
    "ds_read_b64_tr_b16 v[62:63], v7 offset:6656\n\t"
    "v_mfma_f32_32x32x16_bf16 v[32:47], v[68:71], a[84:87], v[32:47]\n\t"
    "ds_read_b64_tr_b16 v[64:65], v7 offset:8192\n\t"
    "ds_read_b64_tr_b16 v[66:67], v7 offset:8704\n\t"
    "v_mfma_f32_32x32x16_bf16 v[32:47], v[72:75], a[88:91], v[32:47]\n\t"
    "ds_read_b64_tr_b16 v[68:69], v7 offset:10240\n\t"
    "ds_read_b64_tr_b16 v[70:71], v7 offset:10752\n\t"
    "v_mfma_f32_32x32x16_bf16 v[32:47], v[76:79], a[92:95], v[32:47]\n\t"
    "ds_read_b64_tr_b16 v[72:73], v7 offset:12288\n\t"
    "ds_read_b64_tr_b16 v[74:75], v7 offset:12800\n\t"
    "s_waitcnt lgkmcnt(12)\n\t"
    "v_mfma_f32_32x32x16_bf16 v[32:47], v[48:51], a[96:99], v[32:47]\n\t"
    "ds_read_b64_tr_b16 v[76:77], v7 offset:14336\n\t"
    "ds_read_b64_tr_b16 v[78:79], v7 offset:14848\n\t"
    "s_waitcnt lgkmcnt(12)\n\t"
    "v_mfma_f32_32x32x16_bf16 v[32:47], v[52:55], a[100:103], v[32:47]\n\t"
    "v_add_u32 v8, 24576, v6\n\t"
    "ds_read_b64_tr_b16 v[48:49], v8 offset:0\n\t"
    "ds_read_b64_tr_b16 v[50:51], v8 offset:512\n\t"
    "s_waitcnt lgkmcnt(12)\n\t"
    "v_mfma_f32_32x32x16_bf16 v[32:47], v[56:59], a[104:107], v[32:47]\n\t"
    "ds_read_b64_tr_b16 v[52:53], v8 offset:2048\n\t"
    "ds_read_b64_tr_b16 v[54:55], v8 offset:2560\n\t"
    "s_waitcnt lgkmcnt(12)\n\t"
    "v_mfma_f32_32x32x16_bf16 v[32:47], v[60:63], a[108:111], v[32:47]\n\t"
    "ds_read_b64_tr_b16 v[56:57], v8 offset:4096\n\t"
    "ds_read_b64_tr_b16 v[58:59], v8 offset:4608\n\t"
    "s_waitcnt lgkmcnt(12)\n\t"
    "v_mfma_f32_32x32x16_bf16 v[32:47], v[64:67], a[112:115], v[32:47]\n\t"
    "ds_read_b64_tr_b16 v[60:61], v8 offset:6144\n\t"
    "ds_read_b64_tr_b16 v[62:63], v8 offset:6656\n\t"
    "s_waitcnt lgkmcnt(12)\n\t"
    "v_mfma_f32_32x32x16_bf16 v[32:47], v[68:71], a[116:119], v[32:47]\n\t"
    "ds_read_b64_tr_b16 v[64:65], v8 offset:8192\n\t"
    "ds_read_b64_tr_b16 v[66:67], v8 offset:8704\n\t"
    "s_waitcnt lgkmcnt(12)\n\t"
    "v_mfma_f32_32x32x16_bf16 v[32:47], v[72:75], a[120:123], v[32:47]\n\t"
    "ds_read_b64_tr_b16 v[68:69], v8 offset:10240\n\t"
    "ds_read_b64_tr_b16 v[70:71], v8 offset:10752\n\t"
    "s_waitcnt lgkmcnt(12)\n\t"
    "v_mfma_f32_32x32x16_bf16 v[32:47], v[76:79], a[124:127], v[32:47]\n\t"
    "ds_read_b64_tr_b16 v[72:73], v8 offset:12288\n\t"
    "ds_read_b64_tr_b16 v[74:75], v8 offset:12800\n\t"
    "s_waitcnt lgkmcnt(12)\n\t"
    "v_mfma_f32_32x32x16_bf16 v[32:47], v[48:51], a[128:131], v[32:47]\n\t"
    "ds_read_b64_tr_b16 v[76:77], v8 offset:14336\n\t"
    "ds_read_b64_tr_b16 v[78:79], v8 offset:14848\n\t"
    "s_waitcnt lgkmcnt(12)\n\t"
    "v_mfma_f32_32x32x16_bf16 v[32:47], v[52:55], a[132:135], v[32:47]\n\t"
    "s_waitcnt lgkmcnt(10)\n\t"
    "v_mfma_f32_32x32x16_bf16 v[32:47], v[56:59], a[136:139], v[32:47]\n\t"
    "s_waitcnt lgkmcnt(8)\n\t"
    "v_mfma_f32_32x32x16_bf16 v[32:47], v[60:63], a[140:143], v[32:47]\n\t"
    "s_waitcnt lgkmcnt(6)\n\t"
    "v_mfma_f32_32x32x16_bf16 v[32:47], v[64:67], a[144:147], v[32:47]\n\t"
    "s_waitcnt lgkmcnt(4)\n\t"
    "v_mfma_f32_32x32x16_bf16 v[32:47], v[68:71], a[148:151], v[32:47]\n\t"
    "s_waitcnt lgkmcnt(2)\n\t"
    "v_mfma_f32_32x32x16_bf16 v[32:47], v[72:75], a[152:155], v[32:47]\n\t"
    "s_waitcnt lgkmcnt(0)\n\t"
    "v_mfma_f32_32x32x16_bf16 v[32:47], v[76:79], a[156:159], v[32:47]\n\t"
    "s_nop 12\n\t"
    "v_maximum3_f32 v32, v32, 0, 0\n\t"
    "v_maximum3_f32 v33, v33, 0, 0\n\t"
    "v_maximum3_f32 v34, v34, 0, 0\n\t"
    "v_maximum3_f32 v35, v35, 0, 0\n\t"
    "v_maximum3_f32 v36, v36, 0, 0\n\t"
    "v_maximum3_f32 v37, v37, 0, 0\n\t"
    "v_maximum3_f32 v38, v38, 0, 0\n\t"
    "v_maximum3_f32 v39, v39, 0, 0\n\t"
    "v_maximum3_f32 v40, v40, 0, 0\n\t"
    "v_maximum3_f32 v41, v41, 0, 0\n\t"
    "v_maximum3_f32 v42, v42, 0, 0\n\t"
    "v_maximum3_f32 v43, v43, 0, 0\n\t"
    "v_maximum3_f32 v44, v44, 0, 0\n\t"
    "v_maximum3_f32 v45, v45, 0, 0\n\t"
    "v_maximum3_f32 v46, v46, 0, 0\n\t"
    "v_maximum3_f32 v47, v47, 0, 0\n\t"
    "v_cvt_pk_bf16_f32 v84, v32, v33\n\t"
    "v_cvt_pk_bf16_f32 v85, v34, v35\n\t"
    "v_cvt_pk_bf16_f32 v86, v36, v37\n\t"
    "v_cvt_pk_bf16_f32 v87, v38, v39\n\t"
    "v_cvt_pk_bf16_f32 v88, v40, v41\n\t"
    "v_cvt_pk_bf16_f32 v89, v42, v43\n\t"
    "v_cvt_pk_bf16_f32 v90, v44, v45\n\t"
    "v_cvt_pk_bf16_f32 v91, v46, v47\n\t"
    "s_lshl_b32 s55, s98, 5\n\t"
    "s_add_u32 s55, s55, s49\n\t"
    "buffer_store_dwordx4 v[84:87], v1, s[16:19], s55 offen sc1\n\t"
    "buffer_store_dwordx4 v[88:91], v1, s[16:19], s55 offen offset:1024 sc1\n\t"
    "s_barrier\n\t"
    "v_add_u32 v8, 81920, v6\n\t"
    "ds_read_b64_tr_b16 v[48:49], v8 offset:0\n\t"
    "ds_read_b64_tr_b16 v[50:51], v8 offset:512\n\t"
    "ds_read_b64_tr_b16 v[52:53], v8 offset:2048\n\t"
    "ds_read_b64_tr_b16 v[54:55], v8 offset:2560\n\t"
    "ds_read_b64_tr_b16 v[56:57], v8 offset:4096\n\t"
    "ds_read_b64_tr_b16 v[58:59], v8 offset:4608\n\t"
    "ds_read_b64_tr_b16 v[60:61], v8 offset:6144\n\t"
    "ds_read_b64_tr_b16 v[62:63], v8 offset:6656\n\t"
    "ds_read_b64_tr_b16 v[64:65], v8 offset:8192\n\t"
    "ds_read_b64_tr_b16 v[66:67], v8 offset:8704\n\t"
    "s_waitcnt lgkmcnt(8)\n\t"
    "v_mfma_f32_32x32x16_bf16 v[16:31], v[48:51], a[32:35], v[16:31]\n\t"
    "ds_read_b64_tr_b16 v[68:69], v8 offset:10240\n\t"
    "ds_read_b64_tr_b16 v[70:71], v8 offset:10752\n\t"
    "s_waitcnt lgkmcnt(8)\n\t"
    "v_mfma_f32_32x32x16_bf16 v[16:31], v[52:55], a[36:39], v[16:31]\n\t"
    "ds_read_b64_tr_b16 v[72:73], v8 offset:12288\n\t"
    "ds_read_b64_tr_b16 v[74:75], v8 offset:12800\n\t"
    "s_waitcnt lgkmcnt(8)\n\t"
    "v_mfma_f32_32x32x16_bf16 v[16:31], v[56:59], a[40:43], v[16:31]\n\t"
    "ds_read_b64_tr_b16 v[76:77], v8 offset:14336\n\t"
    "ds_read_b64_tr_b16 v[78:79], v8 offset:14848\n\t"
    "s_waitcnt lgkmcnt(8)\n\t"
    "v_mfma_f32_32x32x16_bf16 v[16:31], v[60:63], a[44:47], v[16:31]\n\t"
    "v_add_u32 v7, 147456, v6\n\t"
    "ds_read_b64_tr_b16 v[48:49], v7 offset:0\n\t"
    "ds_read_b64_tr_b16 v[50:51], v7 offset:512\n\t"
    "s_waitcnt lgkmcnt(8)\n\t"
    "v_mfma_f32_32x32x16_bf16 v[16:31], v[64:67], a[48:51], v[16:31]\n\t"
    "ds_read_b64_tr_b16 v[52:53], v7 offset:2048\n\t"
    "ds_read_b64_tr_b16 v[54:55], v7 offset:2560\n\t"
    "s_waitcnt lgkmcnt(8)\n\t"
    "v_mfma_f32_32x32x16_bf16 v[16:31], v[68:71], a[52:55], v[16:31]\n\t"
    "ds_read_b64_tr_b16 v[56:57], v7 offset:4096\n\t"
    "ds_read_b64_tr_b16 v[58:59], v7 offset:4608\n\t"
    "s_waitcnt lgkmcnt(8)\n\t"
    "v_mfma_f32_32x32x16_bf16 v[16:31], v[72:75], a[56:59], v[16:31]\n\t"
    "ds_read_b64_tr_b16 v[60:61], v7 offset:6144\n\t"
    "ds_read_b64_tr_b16 v[62:63], v7 offset:6656\n\t"
    "s_waitcnt lgkmcnt(8)\n\t"
    "v_mfma_f32_32x32x16_bf16 v[16:31], v[76:79], a[60:63], v[16:31]\n\t"
    "ds_read_b64_tr_b16 v[64:65], v7 offset:8192\n\t"
    "ds_read_b64_tr_b16 v[66:67], v7 offset:8704\n\t"
    "s_waitcnt lgkmcnt(8)\n\t"
    "v_mfma_f32_32x32x16_bf16 v[16:31], v[48:51], a[64:67], v[16:31]\n\t"
    "ds_read_b64_tr_b16 v[68:69], v7 offset:10240\n\t"
    "ds_read_b64_tr_b16 v[70:71], v7 offset:10752\n\t"
    "s_waitcnt lgkmcnt(8)\n\t"
    "v_mfma_f32_32x32x16_bf16 v[16:31], v[52:55], a[68:71], v[16:31]\n\t"
    "ds_read_b64_tr_b16 v[72:73], v7 offset:12288\n\t"
    "ds_read_b64_tr_b16 v[74:75], v7 offset:12800\n\t"
    "s_waitcnt lgkmcnt(8)\n\t"
    "v_mfma_f32_32x32x16_bf16 v[16:31], v[56:59], a[72:75], v[16:31]\n\t"
    "ds_read_b64_tr_b16 v[76:77], v7 offset:14336\n\t"
    "ds_read_b64_tr_b16 v[78:79], v7 offset:14848\n\t"
    "s_waitcnt lgkmcnt(8)\n\t"
    "v_mfma_f32_32x32x16_bf16 v[16:31], v[60:63], a[76:79], v[16:31]\n\t"
    "s_waitcnt lgkmcnt(6)\n\t"
    "v_mfma_f32_32x32x16_bf16 v[16:31], v[64:67], a[80:83], v[16:31]\n\t"
    "s_waitcnt lgkmcnt(0)\n\t"
    "s_barrier\n\t"
    "s_cmp_lg_u32 s40, 0\n\t"
    "s_cbranch_scc1 L_ap1_i_%=\n\t"
    "s_lshl_b32 s44, s28, 3\n\t"
    "s_add_u32 s44, s44, 4096\n\t"
    "v_lshl_add_u32 v82, v0, 9, s44\n\t"
    "s_cmp_eq_u64 s[58:59], 0\n\t"
    "s_cbranch_scc1 L_ap1_i_%=\n\t"
    "s_mov_b64 exec, s[58:59]\n\t"
    "buffer_load_dwordx2 v[94:95], v82, s[24:27], s57 offen sc1\n\t"
    "s_mov_b64 exec, -1\n\t"
    "L_ap1_i_%=:\n\t"
    "v_add_u32 v8, 65536, v6\n\t"
    "ds_read_b64_tr_b16 v[48:49], v8 offset:0\n\t"
    "ds_read_b64_tr_b16 v[50:51], v8 offset:512\n\t"
    "ds_read_b64_tr_b16 v[52:53], v8 offset:2048\n\t"
    "ds_read_b64_tr_b16 v[54:55], v8 offset:2560\n\t"
    "ds_read_b64_tr_b16 v[56:57], v8 offset:4096\n\t"
    "ds_read_b64_tr_b16 v[58:59], v8 offset:4608\n\t"
    "ds_read_b64_tr_b16 v[60:61], v8 offset:6144\n\t"
    "ds_read_b64_tr_b16 v[62:63], v8 offset:6656\n\t"
    "v_mfma_f32_32x32x16_bf16 v[16:31], v[68:71], a[84:87], v[16:31]\n\t"
    "ds_read_b64_tr_b16 v[64:65], v8 offset:8192\n\t"
    "ds_read_b64_tr_b16 v[66:67], v8 offset:8704\n\t"
    "v_mfma_f32_32x32x16_bf16 v[16:31], v[72:75], a[88:91], v[16:31]\n\t"
    "ds_read_b64_tr_b16 v[68:69], v8 offset:10240\n\t"
    "ds_read_b64_tr_b16 v[70:71], v8 offset:10752\n\t"
    "v_mfma_f32_32x32x16_bf16 v[16:31], v[76:79], a[92:95], v[16:31]\n\t"
    "ds_read_b64_tr_b16 v[72:73], v8 offset:12288\n\t"
    "ds_read_b64_tr_b16 v[74:75], v8 offset:12800\n\t"
    "s_waitcnt lgkmcnt(12)\n\t"
    "v_mfma_f32_32x32x16_bf16 v[16:31], v[48:51], a[96:99], v[16:31]\n\t"
    "ds_read_b64_tr_b16 v[76:77], v8 offset:14336\n\t"
    "ds_read_b64_tr_b16 v[78:79], v8 offset:14848\n\t"
    "s_waitcnt lgkmcnt(12)\n\t"
    "v_mfma_f32_32x32x16_bf16 v[16:31], v[52:55], a[100:103], v[16:31]\n\t"
    "v_add_u32 v7, 49152, v6\n\t"
    "ds_read_b64_tr_b16 v[48:49], v7 offset:0\n\t"
    "ds_read_b64_tr_b16 v[50:51], v7 offset:512\n\t"
    "s_waitcnt lgkmcnt(12)\n\t"
    "v_mfma_f32_32x32x16_bf16 v[16:31], v[56:59], a[104:107], v[16:31]\n\t"
    "ds_read_b64_tr_b16 v[52:53], v7 offset:2048\n\t"
    "ds_read_b64_tr_b16 v[54:55], v7 offset:2560\n\t"
    "s_waitcnt lgkmcnt(12)\n\t"
    "v_mfma_f32_32x32x16_bf16 v[16:31], v[60:63], a[108:111], v[16:31]\n\t"
    "ds_read_b64_tr_b16 v[56:57], v7 offset:4096\n\t"
    "ds_read_b64_tr_b16 v[58:59], v7 offset:4608\n\t"
    "s_waitcnt lgkmcnt(12)\n\t"
    "v_mfma_f32_32x32x16_bf16 v[16:31], v[64:67], a[112:115], v[16:31]\n\t"
    "ds_read_b64_tr_b16 v[60:61], v7 offset:6144\n\t"
    "ds_read_b64_tr_b16 v[62:63], v7 offset:6656\n\t"
    "s_waitcnt lgkmcnt(12)\n\t"
    "v_mfma_f32_32x32x16_bf16 v[16:31], v[68:71], a[116:119], v[16:31]\n\t"
    "ds_read_b64_tr_b16 v[64:65], v7 offset:8192\n\t"
    "ds_read_b64_tr_b16 v[66:67], v7 offset:8704\n\t"
    "s_waitcnt lgkmcnt(12)\n\t"
    "v_mfma_f32_32x32x16_bf16 v[16:31], v[72:75], a[120:123], v[16:31]\n\t"
    "ds_read_b64_tr_b16 v[68:69], v7 offset:10240\n\t"
    "ds_read_b64_tr_b16 v[70:71], v7 offset:10752\n\t"
    "s_waitcnt lgkmcnt(12)\n\t"
    "v_mfma_f32_32x32x16_bf16 v[16:31], v[76:79], a[124:127], v[16:31]\n\t"
    "ds_read_b64_tr_b16 v[72:73], v7 offset:12288\n\t"
    "ds_read_b64_tr_b16 v[74:75], v7 offset:12800\n\t"
    "s_waitcnt lgkmcnt(12)\n\t"
    "v_mfma_f32_32x32x16_bf16 v[16:31], v[48:51], a[128:131], v[16:31]\n\t"
    "ds_read_b64_tr_b16 v[76:77], v7 offset:14336\n\t"
    "ds_read_b64_tr_b16 v[78:79], v7 offset:14848\n\t"
    "s_waitcnt lgkmcnt(12)\n\t"
    "v_mfma_f32_32x32x16_bf16 v[16:31], v[52:55], a[132:135], v[16:31]\n\t"
    "s_waitcnt lgkmcnt(10)\n\t"
    "v_mfma_f32_32x32x16_bf16 v[16:31], v[56:59], a[136:139], v[16:31]\n\t"
    "s_waitcnt lgkmcnt(8)\n\t"
    "v_mfma_f32_32x32x16_bf16 v[16:31], v[60:63], a[140:143], v[16:31]\n\t"
    "s_waitcnt lgkmcnt(6)\n\t"
    "v_mfma_f32_32x32x16_bf16 v[16:31], v[64:67], a[144:147], v[16:31]\n\t"
    "s_waitcnt lgkmcnt(4)\n\t"
    "v_mfma_f32_32x32x16_bf16 v[16:31], v[68:71], a[148:151], v[16:31]\n\t"
    "s_waitcnt lgkmcnt(2)\n\t"
    "v_mfma_f32_32x32x16_bf16 v[16:31], v[72:75], a[152:155], v[16:31]\n\t"
    "s_waitcnt lgkmcnt(0)\n\t"
    "v_mfma_f32_32x32x16_bf16 v[16:31], v[76:79], a[156:159], v[16:31]\n\t"
    "s_waitcnt vmcnt(0)\n\t"
    "s_cmp_lg_u32 s40, 0\n\t"
    "s_cbranch_scc1 L_ap1_done_%=\n\t"
    "s_mov_b32 s62, 0\n\t"
    "s_waitcnt vmcnt(0)\n\t"
    "L_ap1_loop_%=:\n\t"
    "v_subrev_u32 v94, s60, v94\n\t"
    "v_cmp_eq_u32 vcc, s61, v95\n\t"
    "v_add_u32 v94, -1, v94\n\t"
    "v_cmp_ge_u32 s[82:83], 2, v94\n\t"
    "s_and_b64 vcc, vcc, s[82:83]\n\t"
    "s_andn2_b64 s[82:83], s[58:59], vcc\n\t"
    "s_cmp_eq_u64 s[82:83], 0\n\t"
    "s_cbranch_scc1 L_ap1_done_%=\n\t"
    "s_add_u32 s62, s62, 1\n\t"
    "s_cmp_gt_u32 s62, 0x4000\n\t"
    "s_cbranch_scc1 L_ap1_done_%=\n\t"
    "s_and_b32 s44, s62, 3\n\t"
    "s_cmp_lg_u32 s44, 0\n\t"
    "s_cbranch_scc1 L_ap1_same_%=\n\t"
    "v_lshlrev_b32 v82, 3, v0\n\t"
    "buffer_load_dwordx2 v[94:95], v82, s[24:27], s57 offen sc1\n\t"
    "s_waitcnt vmcnt(0)\n\t"
    "v_and_b32 v94, -4, v94\n\t"
    "v_cmp_ne_u64 vcc, v[94:95], v[92:93]\n\t"
    "s_cmp_eq_u64 vcc, 0\n\t"
    "s_cbranch_scc1 L_ap1_same_%=\n\t"
    "v_mov_b32 v92, v94\n\t"
    "v_mov_b32 v93, v95\n\t"
    "v_add_u32 v86, 1, v92\n\t"
    "v_mov_b32 v87, v93\n\t"
    "s_lshl_b32 s44, s28, 9\n\t"
    "s_add_u32 s44, s44, 4096\n\t"
    "v_lshl_add_u32 v83, v0, 3, s44\n\t"
    "buffer_store_dwordx2 v[86:87], v83, s[24:27], s57 offen sc1\n\t"
    "L_ap1_same_%=:\n\t"
    "s_lshl_b32 s44, s28, 3\n\t"
    "s_add_u32 s44, s44, 4096\n\t"
    "v_lshl_add_u32 v82, v0, 9, s44\n\t"
    "buffer_load_dwordx2 v[94:95], v82, s[24:27], s57 offen sc1\n\t"
    "s_waitcnt vmcnt(0)\n\t"
    "s_branch L_ap1_loop_%=\n\t"
    "L_ap1_done_%=:\n\t"
    "s_barrier\n\t"
    "s_cmp_eq_u32 s56, 0\n\t"
    "s_cbranch_scc1 L_cspare_%=\n\t"
    "s_lshr_b32 s43, s30, 1\n\t"
    "s_lshl_b32 s44, s43, 13\n\t"
    "s_xor_b32 s45, s44, 8192\n\t"
    "v_add_u32 v2, s45, v9\n\t"
    "v_add_u32 v9, s44, v9\n\t"
    "s_mul_i32 s44, s43, 12288\n\t"
    "s_sub_u32 s45, 12288, s44\n\t"
    "s_lshl_b32 s43, s43, 11\n\t"
    "v_add_u32 v10, s43, v1\n\t"
    "v_add_u32 v3, s45, v10\n\t"
    "v_add_u32 v10, s44, v10\n\t"
    "s_lshr_b32 s43, s30, 1\n\t"
    "s_cmp_eq_u32 s56, 3\n\t"
    "s_cselect_b32 s43, 2, s43\n\t"
    "s_lshl_b32 s43, s43, 11\n\t"
    "v_add_u32 v11, s43, v1\n\t"
    "v_add_u32 v11, s45, v11\n\t"
    "s_cmp_lg_u32 s40, 0\n\t"
    "s_cbranch_scc1 L_ne2_%=\n\t"
    "v_add_u32 v86, 2, v92\n\t"
    "v_mov_b32 v87, v93\n\t"
    "s_lshl_b32 s44, s28, 9\n\t"
    "s_add_u32 s44, s44, 4096\n\t"
    "v_lshl_add_u32 v83, v0, 3, s44\n\t"
    "buffer_store_dwordx2 v[86:87], v83, s[24:27], s57 offen sc1\n\t"
    "L_ne2_%=:\n\t"
    "v_maximum3_f32 v16, v16, 0, 0\n\t"
    "v_maximum3_f32 v17, v17, 0, 0\n\t"
    "v_maximum3_f32 v18, v18, 0, 0\n\t"
    "v_maximum3_f32 v19, v19, 0, 0\n\t"
    "v_maximum3_f32 v20, v20, 0, 0\n\t"
    "v_maximum3_f32 v21, v21, 0, 0\n\t"
    "v_maximum3_f32 v22, v22, 0, 0\n\t"
    "v_maximum3_f32 v23, v23, 0, 0\n\t"
    "v_maximum3_f32 v24, v24, 0, 0\n\t"
    "v_maximum3_f32 v25, v25, 0, 0\n\t"
    "v_maximum3_f32 v26, v26, 0, 0\n\t"
    "v_maximum3_f32 v27, v27, 0, 0\n\t"
    "v_maximum3_f32 v28, v28, 0, 0\n\t"
    "v_maximum3_f32 v29, v29, 0, 0\n\t"
    "v_maximum3_f32 v30, v30, 0, 0\n\t"
    "v_maximum3_f32 v31, v31, 0, 0\n\t"
    "v_cvt_pk_bf16_f32 v84, v16, v17\n\t"
    "v_cvt_pk_bf16_f32 v85, v18, v19\n\t"
    "v_cvt_pk_bf16_f32 v86, v20, v21\n\t"
    "v_cvt_pk_bf16_f32 v87, v22, v23\n\t"
    "v_cvt_pk_bf16_f32 v88, v24, v25\n\t"
    "v_cvt_pk_bf16_f32 v89, v26, v27\n\t"
    "v_cvt_pk_bf16_f32 v90, v28, v29\n\t"
    "v_cvt_pk_bf16_f32 v91, v30, v31\n\t"
    "s_barrier\n\t"
    "s_lshl_b32 s55, s99, 5\n\t"
    "s_add_u32 s55, s55, s49\n\t"
    "buffer_store_dwordx4 v[84:87], v1, s[16:19], s55 offen sc1\n\t"
    "buffer_store_dwordx4 v[88:91], v1, s[16:19], s55 offen offset:1024 sc1\n\t"
    "s_barrier\n\t"
    "v_add_u32 v12, 0, v9\n\t"
    "v_add_u32 v13, 139264, v10\n\t"
    "v_add_u32 v14, 139264, v11\n\t"
    "v_add_u32 v4, 0, v2\n\t"
    "v_add_u32 v5, 139264, v3\n\t"
    "ds_read_b64_tr_b16 v[48:49], v12 offset:0\n\t"
    "ds_read_b64_tr_b16 v[50:51], v12 offset:1024\n\t"
    "ds_read_b128 a[0:3], v13 offset:0\n\t"
    "ds_read_b64_tr_b16 v[52:53], v12 offset:2048\n\t"
    "ds_read_b64_tr_b16 v[54:55], v12 offset:3072\n\t"
    "ds_read_b128 a[8:11], v13 offset:1024\n\t"
    "ds_read_b64_tr_b16 v[56:57], v12 offset:4096\n\t"
    "ds_read_b64_tr_b16 v[58:59], v12 offset:5120\n\t"
    "ds_read_b128 a[16:19], v13 offset:6144\n\t"
    "ds_read_b64_tr_b16 v[60:61], v12 offset:6144\n\t"
    "ds_read_b64_tr_b16 v[62:63], v12 offset:7168\n\t"
    "ds_read_b128 a[24:27], v13 offset:7168\n\t"
    "ds_read_b64_tr_b16 v[64:65], v4 offset:0\n\t"
    "ds_read_b64_tr_b16 v[66:67], v4 offset:1024\n\t"
    "ds_read_b128 a[32:35], v5 offset:0\n\t"
    "ds_read_b128 a[36:39], v14 offset:0\n\t"
    "s_waitcnt lgkmcnt(13)\n\t"
    "v_mfma_f32_32x32x16_bf16 v[16:31], v[48:51], a[0:3], 0\n\t"
    "ds_read_b64_tr_b16 v[68:69], v4 offset:2048\n\t"
    "ds_read_b64_tr_b16 v[70:71], v4 offset:3072\n\t"
    "ds_read_b128 a[40:43], v5 offset:1024\n\t"
    "ds_read_b128 a[44:47], v14 offset:1024\n\t"
    "s_waitcnt lgkmcnt(14)\n\t"
    "v_mfma_f32_32x32x16_bf16 v[16:31], v[52:55], a[8:11], v[16:31]\n\t"
    "ds_read_b64_tr_b16 v[72:73], v4 offset:4096\n\t"
    "ds_read_b64_tr_b16 v[74:75], v4 offset:5120\n\t"
    "ds_read_b128 a[48:51], v5 offset:6144\n\t"
    "ds_read_b128 a[52:55], v14 offset:6144\n\t"
    "s_waitcnt lgkmcnt(15)\n\t"
    "v_mfma_f32_32x32x16_bf16 v[16:31], v[56:59], a[16:19], v[16:31]\n\t"
    "ds_read_b64_tr_b16 v[76:77], v4 offset:6144\n\t"
    "ds_read_b64_tr_b16 v[78:79], v4 offset:7168\n\t"
    "ds_read_b128 a[56:59], v5 offset:7168\n\t"
    "ds_read_b128 a[60:63], v14 offset:7168\n\t"
    "s_waitcnt lgkmcnt(15)\n\t"
    "v_mfma_f32_32x32x16_bf16 v[16:31], v[60:63], a[24:27], v[16:31]\n\t"
    "s_waitcnt lgkmcnt(12)\n\t"
    "v_mfma_f32_32x32x16_bf16 v[16:31], v[64:67], a[32:35], v[16:31]\n\t"
    "v_mfma_f32_32x32x16_bf16 v[32:47], v[64:67], a[36:39], 0\n\t"
    "s_waitcnt lgkmcnt(0)\n\t"
    "s_barrier\n\t"
    "s_cmp_lg_u32 s40, 0\n\t"
    "s_cbranch_scc1 L_np2_i_%=\n\t"
    "s_lshl_b32 s44, s28, 3\n\t"
    "s_add_u32 s44, s44, 4096\n\t"
    "v_lshl_add_u32 v82, v0, 9, s44\n\t"
    "s_cmp_eq_u64 s[58:59], 0\n\t"
    "s_cbranch_scc1 L_np2_i_%=\n\t"
    "s_mov_b64 exec, s[58:59]\n\t"
    "buffer_load_dwordx2 v[94:95], v82, s[24:27], s57 offen sc1\n\t"
    "s_mov_b64 exec, -1\n\t"
    "L_np2_i_%=:\n\t"
    "v_add_u32 v12, 32768, v9\n\t"
    "v_add_u32 v13, 49152, v10\n\t"
    "v_add_u32 v14, 49152, v11\n\t"
    "v_add_u32 v4, 32768, v2\n\t"
    "v_add_u32 v5, 49152, v3\n\t"
    "ds_read_b64_tr_b16 v[48:49], v12 offset:0\n\t"
    "ds_read_b64_tr_b16 v[50:51], v12 offset:1024\n\t"
    "ds_read_b128 a[0:3], v13 offset:0\n\t"
    "ds_read_b64_tr_b16 v[52:53], v12 offset:2048\n\t"
    "ds_read_b64_tr_b16 v[54:55], v12 offset:3072\n\t"
    "ds_read_b128 a[8:11], v13 offset:1024\n\t"
    "ds_read_b64_tr_b16 v[56:57], v12 offset:4096\n\t"
    "ds_read_b64_tr_b16 v[58:59], v12 offset:5120\n\t"
    "ds_read_b128 a[16:19], v13 offset:6144\n\t"
    "ds_read_b64_tr_b16 v[60:61], v12 offset:6144\n\t"
    "ds_read_b64_tr_b16 v[62:63], v12 offset:7168\n\t"
    "ds_read_b128 a[24:27], v13 offset:7168\n\t"
    "v_mfma_f32_32x32x16_bf16 v[16:31], v[68:71], a[40:43], v[16:31]\n\t"
    "v_mfma_f32_32x32x16_bf16 v[32:47], v[68:71], a[44:47], v[32:47]\n\t"
    "ds_read_b64_tr_b16 v[64:65], v4 offset:0\n\t"
    "ds_read_b64_tr_b16 v[66:67], v4 offset:1024\n\t"
    "ds_read_b128 a[32:35], v5 offset:0\n\t"
    "ds_read_b128 a[36:39], v14 offset:0\n\t"
    "v_mfma_f32_32x32x16_bf16 v[16:31], v[72:75], a[48:51], v[16:31]\n\t"
    "v_mfma_f32_32x32x16_bf16 v[32:47], v[72:75], a[52:55], v[32:47]\n\t"
    "ds_read_b64_tr_b16 v[68:69], v4 offset:2048\n\t"
    "ds_read_b64_tr_b16 v[70:71], v4 offset:3072\n\t"
    "ds_read_b128 a[40:43], v5 offset:1024\n\t"
    "ds_read_b128 a[44:47], v14 offset:1024\n\t"
    "v_mfma_f32_32x32x16_bf16 v[16:31], v[76:79], a[56:59], v[16:31]\n\t"
    "v_mfma_f32_32x32x16_bf16 v[32:47], v[76:79], a[60:63], v[32:47]\n\t"
    "ds_read_b64_tr_b16 v[72:73], v4 offset:4096\n\t"
    "ds_read_b64_tr_b16 v[74:75], v4 offset:5120\n\t"
    "ds_read_b128 a[48:51], v5 offset:6144\n\t"
    "ds_read_b128 a[52:55], v14 offset:6144\n\t"
    "s_waitcnt lgkmcnt(15)\n\t"
    "v_mfma_f32_32x32x16_bf16 v[16:31], v[48:51], a[0:3], v[16:31]\n\t"
    "ds_read_b64_tr_b16 v[76:77], v4 offset:6144\n\t"
    "ds_read_b64_tr_b16 v[78:79], v4 offset:7168\n\t"
    "ds_read_b128 a[56:59], v5 offset:7168\n\t"
    "ds_read_b128 a[60:63], v14 offset:7168\n\t"
    "v_mfma_f32_32x32x16_bf16 v[16:31], v[52:55], a[8:11], v[16:31]\n\t"
    "v_mfma_f32_32x32x16_bf16 v[16:31], v[56:59], a[16:19], v[16:31]\n\t"
    "s_waitcnt lgkmcnt(15)\n\t"
    "v_mfma_f32_32x32x16_bf16 v[16:31], v[60:63], a[24:27], v[16:31]\n\t"
    "s_waitcnt lgkmcnt(12)\n\t"
    "v_mfma_f32_32x32x16_bf16 v[16:31], v[64:67], a[32:35], v[16:31]\n\t"
    "v_mfma_f32_32x32x16_bf16 v[32:47], v[64:67], a[36:39], v[32:47]\n\t"
    "s_waitcnt vmcnt(0)\n\t"
    "s_cmp_lg_u32 s40, 0\n\t"
    "s_cbranch_scc1 L_np2_done_%=\n\t"
    "s_mov_b32 s62, 0\n\t"
    "s_waitcnt vmcnt(0)\n\t"
    "L_np2_loop_%=:\n\t"
    "v_subrev_u32 v94, s60, v94\n\t"
    "v_cmp_eq_u32 vcc, s61, v95\n\t"
    "v_add_u32 v94, -1, v94\n\t"
    "v_cmp_ge_u32 s[82:83], 2, v94\n\t"
    "s_and_b64 vcc, vcc, s[82:83]\n\t"
    "v_cmp_le_u32 s[82:83], 1, v94\n\t"
    "s_and_b64 s[82:83], s[82:83], vcc\n\t"
    "s_andn2_b64 s[82:83], s[58:59], s[82:83]\n\t"
    "s_cmp_eq_u64 s[82:83], 0\n\t"
    "s_cbranch_scc1 L_np2_done_%=\n\t"
    "s_add_u32 s62, s62, 1\n\t"
    "s_cmp_gt_u32 s62, 0x4000\n\t"
    "s_cbranch_scc1 L_np2_done_%=\n\t"
    "s_and_b32 s44, s62, 3\n\t"
    "s_cmp_lg_u32 s44, 0\n\t"
    "s_cbranch_scc1 L_np2_same_%=\n\t"
    "v_lshlrev_b32 v82, 3, v0\n\t"
    "buffer_load_dwordx2 v[94:95], v82, s[24:27], s57 offen sc1\n\t"
    "s_waitcnt vmcnt(0)\n\t"
    "v_and_b32 v94, -4, v94\n\t"
    "v_cmp_ne_u64 vcc, v[94:95], v[92:93]\n\t"
    "s_cmp_eq_u64 vcc, 0\n\t"
    "s_cbranch_scc1 L_np2_same_%=\n\t"
    "v_mov_b32 v92, v94\n\t"
    "v_mov_b32 v93, v95\n\t"
    "v_add_u32 v86, 2, v92\n\t"
    "v_mov_b32 v87, v93\n\t"
    "s_lshl_b32 s44, s28, 9\n\t"
    "s_add_u32 s44, s44, 4096\n\t"
    "v_lshl_add_u32 v83, v0, 3, s44\n\t"
    "buffer_store_dwordx2 v[86:87], v83, s[24:27], s57 offen sc1\n\t"
    "L_np2_same_%=:\n\t"
    "s_lshl_b32 s44, s28, 3\n\t"
    "s_add_u32 s44, s44, 4096\n\t"
    "v_lshl_add_u32 v82, v0, 9, s44\n\t"
    "buffer_load_dwordx2 v[94:95], v82, s[24:27], s57 offen sc1\n\t"
    "s_waitcnt vmcnt(0)\n\t"
    "s_branch L_np2_loop_%=\n\t"
    "L_np2_done_%=:\n\t"
    "s_waitcnt lgkmcnt(0)\n\t"
    "s_barrier\n\t"
    "s_cmp_lg_u32 s40, 0\n\t"
    "s_cbranch_scc1 L_ne3_%=\n\t"
    "v_add_u32 v86, 3, v92\n\t"
    "v_mov_b32 v87, v93\n\t"
    "s_lshl_b32 s44, s28, 9\n\t"
    "s_add_u32 s44, s44, 4096\n\t"
    "v_lshl_add_u32 v83, v0, 3, s44\n\t"
    "buffer_store_dwordx2 v[86:87], v83, s[24:27], s57 offen sc1\n\t"
    "L_ne3_%=:\n\t"
    "v_add_u32 v12, 122880, v9\n\t"
    "v_add_u32 v13, 98304, v10\n\t"
    "v_add_u32 v14, 98304, v11\n\t"
    "v_add_u32 v4, 122880, v2\n\t"
    "v_add_u32 v5, 98304, v3\n\t"
    "ds_read_b64_tr_b16 v[48:49], v12 offset:0\n\t"
    "ds_read_b64_tr_b16 v[50:51], v12 offset:1024\n\t"
    "ds_read_b128 a[0:3], v13 offset:0\n\t"
    "ds_read_b64_tr_b16 v[52:53], v12 offset:2048\n\t"
    "ds_read_b64_tr_b16 v[54:55], v12 offset:3072\n\t"
    "ds_read_b128 a[8:11], v13 offset:1024\n\t"
    "ds_read_b64_tr_b16 v[56:57], v12 offset:4096\n\t"
    "ds_read_b64_tr_b16 v[58:59], v12 offset:5120\n\t"
    "ds_read_b128 a[16:19], v13 offset:6144\n\t"
    "ds_read_b64_tr_b16 v[60:61], v12 offset:6144\n\t"
    "ds_read_b64_tr_b16 v[62:63], v12 offset:7168\n\t"
    "ds_read_b128 a[24:27], v13 offset:7168\n\t"
    "v_mfma_f32_32x32x16_bf16 v[16:31], v[68:71], a[40:43], v[16:31]\n\t"
    "v_mfma_f32_32x32x16_bf16 v[32:47], v[68:71], a[44:47], v[32:47]\n\t"
    "ds_read_b64_tr_b16 v[64:65], v4 offset:0\n\t"
    "ds_read_b64_tr_b16 v[66:67], v4 offset:1024\n\t"
    "ds_read_b128 a[32:35], v5 offset:0\n\t"
    "ds_read_b128 a[36:39], v14 offset:0\n\t"
    "v_mfma_f32_32x32x16_bf16 v[16:31], v[72:75], a[48:51], v[16:31]\n\t"
    "v_mfma_f32_32x32x16_bf16 v[32:47], v[72:75], a[52:55], v[32:47]\n\t"
    "ds_read_b64_tr_b16 v[68:69], v4 offset:2048\n\t"
    "ds_read_b64_tr_b16 v[70:71], v4 offset:3072\n\t"
    "ds_read_b128 a[40:43], v5 offset:1024\n\t"
    "ds_read_b128 a[44:47], v14 offset:1024\n\t"
    "v_mfma_f32_32x32x16_bf16 v[16:31], v[76:79], a[56:59], v[16:31]\n\t"
    "v_mfma_f32_32x32x16_bf16 v[32:47], v[76:79], a[60:63], v[32:47]\n\t"
    "ds_read_b64_tr_b16 v[72:73], v4 offset:4096\n\t"
    "ds_read_b64_tr_b16 v[74:75], v4 offset:5120\n\t"
    "ds_read_b128 a[48:51], v5 offset:6144\n\t"
    "ds_read_b128 a[52:55], v14 offset:6144\n\t"
    "s_waitcnt lgkmcnt(15)\n\t"
    "v_mfma_f32_32x32x16_bf16 v[16:31], v[48:51], a[0:3], v[16:31]\n\t"
    "ds_read_b64_tr_b16 v[76:77], v4 offset:6144\n\t"
    "ds_read_b64_tr_b16 v[78:79], v4 offset:7168\n\t"
    "ds_read_b128 a[56:59], v5 offset:7168\n\t"
    "ds_read_b128 a[60:63], v14 offset:7168\n\t"
    "v_mfma_f32_32x32x16_bf16 v[16:31], v[52:55], a[8:11], v[16:31]\n\t"
    "v_mfma_f32_32x32x16_bf16 v[16:31], v[56:59], a[16:19], v[16:31]\n\t"
    "s_waitcnt lgkmcnt(15)\n\t"
    "v_mfma_f32_32x32x16_bf16 v[16:31], v[60:63], a[24:27], v[16:31]\n\t"
    "s_waitcnt lgkmcnt(12)\n\t"
    "v_mfma_f32_32x32x16_bf16 v[16:31], v[64:67], a[32:35], v[16:31]\n\t"
    "v_mfma_f32_32x32x16_bf16 v[32:47], v[64:67], a[36:39], v[32:47]\n\t"
    "s_waitcnt lgkmcnt(0)\n\t"
    "s_barrier\n\t"
    "v_add_u32 v12, 16384, v9\n\t"
    "v_add_u32 v13, 139264, v10\n\t"
    "v_add_u32 v14, 139264, v11\n\t"
    "v_add_u32 v4, 16384, v2\n\t"
    "v_add_u32 v5, 139264, v3\n\t"
    "ds_read_b64_tr_b16 v[48:49], v12 offset:0\n\t"
    "ds_read_b64_tr_b16 v[50:51], v12 offset:1024\n\t"
    "ds_read_b128 a[0:3], v13 offset:0\n\t"
    "ds_read_b64_tr_b16 v[52:53], v12 offset:2048\n\t"
    "ds_read_b64_tr_b16 v[54:55], v12 offset:3072\n\t"
    "ds_read_b128 a[8:11], v13 offset:1024\n\t"
    "ds_read_b64_tr_b16 v[56:57], v12 offset:4096\n\t"
    "ds_read_b64_tr_b16 v[58:59], v12 offset:5120\n\t"
    "ds_read_b128 a[16:19], v13 offset:6144\n\t"
    "ds_read_b64_tr_b16 v[60:61], v12 offset:6144\n\t"
    "ds_read_b64_tr_b16 v[62:63], v12 offset:7168\n\t"
    "ds_read_b128 a[24:27], v13 offset:7168\n\t"
    "v_mfma_f32_32x32x16_bf16 v[16:31], v[68:71], a[40:43], v[16:31]\n\t"
    "v_mfma_f32_32x32x16_bf16 v[32:47], v[68:71], a[44:47], v[32:47]\n\t"
    "ds_read_b64_tr_b16 v[64:65], v4 offset:0\n\t"
    "ds_read_b64_tr_b16 v[66:67], v4 offset:1024\n\t"
    "ds_read_b128 a[32:35], v5 offset:0\n\t"
    "ds_read_b128 a[36:39], v14 offset:0\n\t"
    "v_mfma_f32_32x32x16_bf16 v[16:31], v[72:75], a[48:51], v[16:31]\n\t"
    "v_mfma_f32_32x32x16_bf16 v[32:47], v[72:75], a[52:55], v[32:47]\n\t"
    "ds_read_b64_tr_b16 v[68:69], v4 offset:2048\n\t"
    "ds_read_b64_tr_b16 v[70:71], v4 offset:3072\n\t"
    "ds_read_b128 a[40:43], v5 offset:1024\n\t"
    "ds_read_b128 a[44:47], v14 offset:1024\n\t"
    "v_mfma_f32_32x32x16_bf16 v[16:31], v[76:79], a[56:59], v[16:31]\n\t"
    "v_mfma_f32_32x32x16_bf16 v[32:47], v[76:79], a[60:63], v[32:47]\n\t"
    "ds_read_b64_tr_b16 v[72:73], v4 offset:4096\n\t"
    "ds_read_b64_tr_b16 v[74:75], v4 offset:5120\n\t"
    "ds_read_b128 a[48:51], v5 offset:6144\n\t"
    "ds_read_b128 a[52:55], v14 offset:6144\n\t"
    "s_waitcnt lgkmcnt(15)\n\t"
    "v_mfma_f32_32x32x16_bf16 v[16:31], v[48:51], a[0:3], v[16:31]\n\t"
    "ds_read_b64_tr_b16 v[76:77], v4 offset:6144\n\t"
    "ds_read_b64_tr_b16 v[78:79], v4 offset:7168\n\t"
    "ds_read_b128 a[56:59], v5 offset:7168\n\t"
    "ds_read_b128 a[60:63], v14 offset:7168\n\t"
    "v_mfma_f32_32x32x16_bf16 v[16:31], v[52:55], a[8:11], v[16:31]\n\t"
    "v_mfma_f32_32x32x16_bf16 v[16:31], v[56:59], a[16:19], v[16:31]\n\t"
    "s_waitcnt lgkmcnt(15)\n\t"
    "v_mfma_f32_32x32x16_bf16 v[16:31], v[60:63], a[24:27], v[16:31]\n\t"
    "s_waitcnt lgkmcnt(12)\n\t"
    "v_mfma_f32_32x32x16_bf16 v[16:31], v[64:67], a[32:35], v[16:31]\n\t"
    "v_mfma_f32_32x32x16_bf16 v[32:47], v[64:67], a[36:39], v[32:47]\n\t"
    "s_waitcnt lgkmcnt(0)\n\t"
    "s_barrier\n\t"
    "v_add_u32 v12, 81920, v9\n\t"
    "v_add_u32 v13, 32768, v10\n\t"
    "v_add_u32 v14, 32768, v11\n\t"
    "v_add_u32 v4, 81920, v2\n\t"
    "v_add_u32 v5, 32768, v3\n\t"
    "ds_read_b64_tr_b16 v[48:49], v12 offset:0\n\t"
    "ds_read_b64_tr_b16 v[50:51], v12 offset:1024\n\t"
    "ds_read_b128 a[0:3], v13 offset:0\n\t"
    "ds_read_b64_tr_b16 v[52:53], v12 offset:2048\n\t"
    "ds_read_b64_tr_b16 v[54:55], v12 offset:3072\n\t"
    "ds_read_b128 a[8:11], v13 offset:1024\n\t"
    "ds_read_b64_tr_b16 v[56:57], v12 offset:4096\n\t"
    "ds_read_b64_tr_b16 v[58:59], v12 offset:5120\n\t"
    "ds_read_b128 a[16:19], v13 offset:6144\n\t"
    "ds_read_b64_tr_b16 v[60:61], v12 offset:6144\n\t"
    "ds_read_b64_tr_b16 v[62:63], v12 offset:7168\n\t"
    "ds_read_b128 a[24:27], v13 offset:7168\n\t"
    "v_mfma_f32_32x32x16_bf16 v[16:31], v[68:71], a[40:43], v[16:31]\n\t"
    "v_mfma_f32_32x32x16_bf16 v[32:47], v[68:71], a[44:47], v[32:47]\n\t"
    "ds_read_b64_tr_b16 v[64:65], v4 offset:0\n\t"
    "ds_read_b64_tr_b16 v[66:67], v4 offset:1024\n\t"
    "ds_read_b128 a[32:35], v5 offset:0\n\t"
    "ds_read_b128 a[36:39], v14 offset:0\n\t"
    "v_mfma_f32_32x32x16_bf16 v[16:31], v[72:75], a[48:51], v[16:31]\n\t"
    "v_mfma_f32_32x32x16_bf16 v[32:47], v[72:75], a[52:55], v[32:47]\n\t"
    "ds_read_b64_tr_b16 v[68:69], v4 offset:2048\n\t"
    "ds_read_b64_tr_b16 v[70:71], v4 offset:3072\n\t"
    "ds_read_b128 a[40:43], v5 offset:1024\n\t"
    "ds_read_b128 a[44:47], v14 offset:1024\n\t"
    "v_mfma_f32_32x32x16_bf16 v[16:31], v[76:79], a[56:59], v[16:31]\n\t"
    "v_mfma_f32_32x32x16_bf16 v[32:47], v[76:79], a[60:63], v[32:47]\n\t"
    "ds_read_b64_tr_b16 v[72:73], v4 offset:4096\n\t"
    "ds_read_b64_tr_b16 v[74:75], v4 offset:5120\n\t"
    "ds_read_b128 a[48:51], v5 offset:6144\n\t"
    "ds_read_b128 a[52:55], v14 offset:6144\n\t"
    "s_waitcnt lgkmcnt(15)\n\t"
    "v_mfma_f32_32x32x16_bf16 v[16:31], v[48:51], a[0:3], v[16:31]\n\t"
    "ds_read_b64_tr_b16 v[76:77], v4 offset:6144\n\t"
    "ds_read_b64_tr_b16 v[78:79], v4 offset:7168\n\t"
    "ds_read_b128 a[56:59], v5 offset:7168\n\t"
    "ds_read_b128 a[60:63], v14 offset:7168\n\t"
    "v_mfma_f32_32x32x16_bf16 v[16:31], v[52:55], a[8:11], v[16:31]\n\t"
    "v_mfma_f32_32x32x16_bf16 v[16:31], v[56:59], a[16:19], v[16:31]\n\t"
    "s_waitcnt lgkmcnt(15)\n\t"
    "v_mfma_f32_32x32x16_bf16 v[16:31], v[60:63], a[24:27], v[16:31]\n\t"
    "s_waitcnt lgkmcnt(12)\n\t"
    "v_mfma_f32_32x32x16_bf16 v[16:31], v[64:67], a[32:35], v[16:31]\n\t"
    "v_mfma_f32_32x32x16_bf16 v[32:47], v[64:67], a[36:39], v[32:47]\n\t"
    "s_waitcnt lgkmcnt(0)\n\t"
    "s_barrier\n\t"
    "s_cmp_lg_u32 s40, 0\n\t"
    "s_cbranch_scc1 L_np3_i_%=\n\t"
    "s_lshl_b32 s44, s28, 3\n\t"
    "s_add_u32 s44, s44, 4096\n\t"
    "v_lshl_add_u32 v82, v0, 9, s44\n\t"
    "s_cmp_eq_u64 s[58:59], 0\n\t"
    "s_cbranch_scc1 L_np3_i_%=\n\t"
    "s_mov_b64 exec, s[58:59]\n\t"
    "buffer_load_dwordx2 v[94:95], v82, s[24:27], s57 offen sc1\n\t"
    "s_mov_b64 exec, -1\n\t"
    "L_np3_i_%=:\n\t"
    "v_add_u32 v12, 0, v9\n\t"
    "v_add_u32 v13, 98304, v10\n\t"
    "v_add_u32 v14, 98304, v11\n\t"
    "v_add_u32 v4, 0, v2\n\t"
    "v_add_u32 v5, 98304, v3\n\t"
    "ds_read_b64_tr_b16 v[48:49], v12 offset:0\n\t"
    "ds_read_b64_tr_b16 v[50:51], v12 offset:1024\n\t"
    "ds_read_b128 a[0:3], v13 offset:0\n\t"
    "ds_read_b64_tr_b16 v[52:53], v12 offset:2048\n\t"
    "ds_read_b64_tr_b16 v[54:55], v12 offset:3072\n\t"
    "ds_read_b128 a[8:11], v13 offset:1024\n\t"
    "ds_read_b64_tr_b16 v[56:57], v12 offset:4096\n\t"
    "ds_read_b64_tr_b16 v[58:59], v12 offset:5120\n\t"
    "ds_read_b128 a[16:19], v13 offset:6144\n\t"
    "ds_read_b64_tr_b16 v[60:61], v12 offset:6144\n\t"
    "ds_read_b64_tr_b16 v[62:63], v12 offset:7168\n\t"
    "ds_read_b128 a[24:27], v13 offset:7168\n\t"
    "v_mfma_f32_32x32x16_bf16 v[16:31], v[68:71], a[40:43], v[16:31]\n\t"
    "v_mfma_f32_32x32x16_bf16 v[32:47], v[68:71], a[44:47], v[32:47]\n\t"
    "ds_read_b64_tr_b16 v[64:65], v4 offset:0\n\t"
    "ds_read_b64_tr_b16 v[66:67], v4 offset:1024\n\t"
    "ds_read_b128 a[32:35], v5 offset:0\n\t"
    "ds_read_b128 a[36:39], v14 offset:0\n\t"
    "v_mfma_f32_32x32x16_bf16 v[16:31], v[72:75], a[48:51], v[16:31]\n\t"
    "v_mfma_f32_32x32x16_bf16 v[32:47], v[72:75], a[52:55], v[32:47]\n\t"
    "ds_read_b64_tr_b16 v[68:69], v4 offset:2048\n\t"
    "ds_read_b64_tr_b16 v[70:71], v4 offset:3072\n\t"
    "ds_read_b128 a[40:43], v5 offset:1024\n\t"
    "ds_read_b128 a[44:47], v14 offset:1024\n\t"
    "v_mfma_f32_32x32x16_bf16 v[16:31], v[76:79], a[56:59], v[16:31]\n\t"
    "v_mfma_f32_32x32x16_bf16 v[32:47], v[76:79], a[60:63], v[32:47]\n\t"
    "ds_read_b64_tr_b16 v[72:73], v4 offset:4096\n\t"
    "ds_read_b64_tr_b16 v[74:75], v4 offset:5120\n\t"
    "ds_read_b128 a[48:51], v5 offset:6144\n\t"
    "ds_read_b128 a[52:55], v14 offset:6144\n\t"
    "s_waitcnt lgkmcnt(15)\n\t"
    "v_mfma_f32_32x32x16_bf16 v[16:31], v[48:51], a[0:3], v[16:31]\n\t"
    "ds_read_b64_tr_b16 v[76:77], v4 offset:6144\n\t"
    "ds_read_b64_tr_b16 v[78:79], v4 offset:7168\n\t"
    "ds_read_b128 a[56:59], v5 offset:7168\n\t"
    "ds_read_b128 a[60:63], v14 offset:7168\n\t"
    "v_mfma_f32_32x32x16_bf16 v[16:31], v[52:55], a[8:11], v[16:31]\n\t"
    "v_mfma_f32_32x32x16_bf16 v[16:31], v[56:59], a[16:19], v[16:31]\n\t"
    "s_waitcnt lgkmcnt(15)\n\t"
    "v_mfma_f32_32x32x16_bf16 v[16:31], v[60:63], a[24:27], v[16:31]\n\t"
    "s_waitcnt lgkmcnt(12)\n\t"
    "v_mfma_f32_32x32x16_bf16 v[16:31], v[64:67], a[32:35], v[16:31]\n\t"
    "v_mfma_f32_32x32x16_bf16 v[32:47], v[64:67], a[36:39], v[32:47]\n\t"
    "s_cmp_lg_u32 s40, 0\n\t"
    "s_cbranch_scc1 L_np3_done_%=\n\t"
    "s_mov_b32 s62, 0\n\t"
    "s_waitcnt vmcnt(0)\n\t"
    "L_np3_loop_%=:\n\t"
    "v_subrev_u32 v94, s60, v94\n\t"
    "v_cmp_eq_u32 vcc, s61, v95\n\t"
    "v_add_u32 v94, -1, v94\n\t"
    "v_cmp_ge_u32 s[82:83], 2, v94\n\t"
    "s_and_b64 vcc, vcc, s[82:83]\n\t"
    "v_cmp_le_u32 s[82:83], 2, v94\n\t"
    "s_and_b64 s[82:83], s[82:83], vcc\n\t"
    "s_andn2_b64 s[82:83], s[58:59], s[82:83]\n\t"
    "s_cmp_eq_u64 s[82:83], 0\n\t"
    "s_cbranch_scc1 L_np3_done_%=\n\t"
    "s_add_u32 s62, s62, 1\n\t"
    "s_cmp_gt_u32 s62, 0x4000\n\t"
    "s_cbranch_scc1 L_np3_done_%=\n\t"
    "s_and_b32 s44, s62, 3\n\t"
    "s_cmp_lg_u32 s44, 0\n\t"
    "s_cbranch_scc1 L_np3_same_%=\n\t"
    "v_lshlrev_b32 v82, 3, v0\n\t"
    "buffer_load_dwordx2 v[94:95], v82, s[24:27], s57 offen sc1\n\t"
    "s_waitcnt vmcnt(0)\n\t"
    "v_and_b32 v94, -4, v94\n\t"
    "v_cmp_ne_u64 vcc, v[94:95], v[92:93]\n\t"
    "s_cmp_eq_u64 vcc, 0\n\t"
    "s_cbranch_scc1 L_np3_same_%=\n\t"
    "v_mov_b32 v92, v94\n\t"
    "v_mov_b32 v93, v95\n\t"
    "v_add_u32 v86, 3, v92\n\t"
    "v_mov_b32 v87, v93\n\t"
    "s_lshl_b32 s44, s28, 9\n\t"
    "s_add_u32 s44, s44, 4096\n\t"
    "v_lshl_add_u32 v83, v0, 3, s44\n\t"
    "buffer_store_dwordx2 v[86:87], v83, s[24:27], s57 offen sc1\n\t"
    "L_np3_same_%=:\n\t"
    "s_lshl_b32 s44, s28, 3\n\t"
    "s_add_u32 s44, s44, 4096\n\t"
    "v_lshl_add_u32 v82, v0, 9, s44\n\t"
    "buffer_load_dwordx2 v[94:95], v82, s[24:27], s57 offen sc1\n\t"
    "s_waitcnt vmcnt(0)\n\t"
    "s_branch L_np3_loop_%=\n\t"
    "L_np3_done_%=:\n\t"
    "s_waitcnt lgkmcnt(0)\n\t"
    "s_barrier\n\t"
    "v_add_u32 v12, 57344, v9\n\t"
    "v_add_u32 v13, 139264, v10\n\t"
    "v_add_u32 v14, 139264, v11\n\t"
    "v_add_u32 v4, 57344, v2\n\t"
    "v_add_u32 v5, 139264, v3\n\t"
    "ds_read_b64_tr_b16 v[48:49], v12 offset:0\n\t"
    "ds_read_b64_tr_b16 v[50:51], v12 offset:1024\n\t"
    "ds_read_b128 a[0:3], v13 offset:0\n\t"
    "ds_read_b64_tr_b16 v[52:53], v12 offset:2048\n\t"
    "ds_read_b64_tr_b16 v[54:55], v12 offset:3072\n\t"
    "ds_read_b128 a[8:11], v13 offset:1024\n\t"
    "ds_read_b64_tr_b16 v[56:57], v12 offset:4096\n\t"
    "ds_read_b64_tr_b16 v[58:59], v12 offset:5120\n\t"
    "ds_read_b128 a[16:19], v13 offset:6144\n\t"
    "ds_read_b64_tr_b16 v[60:61], v12 offset:6144\n\t"
    "ds_read_b64_tr_b16 v[62:63], v12 offset:7168\n\t"
    "ds_read_b128 a[24:27], v13 offset:7168\n\t"
    "v_mfma_f32_32x32x16_bf16 v[16:31], v[68:71], a[40:43], v[16:31]\n\t"
    "v_mfma_f32_32x32x16_bf16 v[32:47], v[68:71], a[44:47], v[32:47]\n\t"
    "ds_read_b64_tr_b16 v[64:65], v4 offset:0\n\t"
    "ds_read_b64_tr_b16 v[66:67], v4 offset:1024\n\t"
    "ds_read_b128 a[32:35], v5 offset:0\n\t"
    "ds_read_b128 a[36:39], v14 offset:0\n\t"
    "v_mfma_f32_32x32x16_bf16 v[16:31], v[72:75], a[48:51], v[16:31]\n\t"
    "v_mfma_f32_32x32x16_bf16 v[32:47], v[72:75], a[52:55], v[32:47]\n\t"
    "ds_read_b64_tr_b16 v[68:69], v4 offset:2048\n\t"
    "ds_read_b64_tr_b16 v[70:71], v4 offset:3072\n\t"
    "ds_read_b128 a[40:43], v5 offset:1024\n\t"
    "ds_read_b128 a[44:47], v14 offset:1024\n\t"
    "v_mfma_f32_32x32x16_bf16 v[16:31], v[76:79], a[56:59], v[16:31]\n\t"
    "v_mfma_f32_32x32x16_bf16 v[32:47], v[76:79], a[60:63], v[32:47]\n\t"
    "ds_read_b64_tr_b16 v[72:73], v4 offset:4096\n\t"
    "ds_read_b64_tr_b16 v[74:75], v4 offset:5120\n\t"
    "ds_read_b128 a[48:51], v5 offset:6144\n\t"
    "ds_read_b128 a[52:55], v14 offset:6144\n\t"
    "s_waitcnt lgkmcnt(15)\n\t"
    "v_mfma_f32_32x32x16_bf16 v[16:31], v[48:51], a[0:3], v[16:31]\n\t"
    "ds_read_b64_tr_b16 v[76:77], v4 offset:6144\n\t"
    "ds_read_b64_tr_b16 v[78:79], v4 offset:7168\n\t"
    "ds_read_b128 a[56:59], v5 offset:7168\n\t"
    "ds_read_b128 a[60:63], v14 offset:7168\n\t"
    "v_mfma_f32_32x32x16_bf16 v[16:31], v[52:55], a[8:11], v[16:31]\n\t"
    "v_mfma_f32_32x32x16_bf16 v[16:31], v[56:59], a[16:19], v[16:31]\n\t"
    "s_waitcnt lgkmcnt(15)\n\t"
    "v_mfma_f32_32x32x16_bf16 v[16:31], v[60:63], a[24:27], v[16:31]\n\t"
    "s_waitcnt lgkmcnt(12)\n\t"
    "v_mfma_f32_32x32x16_bf16 v[16:31], v[64:67], a[32:35], v[16:31]\n\t"
    "v_mfma_f32_32x32x16_bf16 v[32:47], v[64:67], a[36:39], v[32:47]\n\t"
    "s_waitcnt lgkmcnt(0)\n\t"
    "s_barrier\n\t"
    "v_add_u32 v12, 122880, v9\n\t"
    "v_add_u32 v13, 32768, v10\n\t"
    "v_add_u32 v14, 32768, v11\n\t"
    "v_add_u32 v4, 122880, v2\n\t"
    "v_add_u32 v5, 32768, v3\n\t"
    "ds_read_b64_tr_b16 v[48:49], v12 offset:0\n\t"
    "ds_read_b64_tr_b16 v[50:51], v12 offset:1024\n\t"
    "ds_read_b128 a[0:3], v13 offset:0\n\t"
    "ds_read_b64_tr_b16 v[52:53], v12 offset:2048\n\t"
    "ds_read_b64_tr_b16 v[54:55], v12 offset:3072\n\t"
    "ds_read_b128 a[8:11], v13 offset:1024\n\t"
    "ds_read_b64_tr_b16 v[56:57], v12 offset:4096\n\t"
    "ds_read_b64_tr_b16 v[58:59], v12 offset:5120\n\t"
    "ds_read_b128 a[16:19], v13 offset:6144\n\t"
    "ds_read_b64_tr_b16 v[60:61], v12 offset:6144\n\t"
    "ds_read_b64_tr_b16 v[62:63], v12 offset:7168\n\t"
    "ds_read_b128 a[24:27], v13 offset:7168\n\t"
    "v_mfma_f32_32x32x16_bf16 v[16:31], v[68:71], a[40:43], v[16:31]\n\t"
    "v_mfma_f32_32x32x16_bf16 v[32:47], v[68:71], a[44:47], v[32:47]\n\t"
    "ds_read_b64_tr_b16 v[64:65], v4 offset:0\n\t"
    "ds_read_b64_tr_b16 v[66:67], v4 offset:1024\n\t"
    "ds_read_b128 a[32:35], v5 offset:0\n\t"
    "ds_read_b128 a[36:39], v14 offset:0\n\t"
    "v_mfma_f32_32x32x16_bf16 v[16:31], v[72:75], a[48:51], v[16:31]\n\t"
    "v_mfma_f32_32x32x16_bf16 v[32:47], v[72:75], a[52:55], v[32:47]\n\t"
    "ds_read_b64_tr_b16 v[68:69], v4 offset:2048\n\t"
    "ds_read_b64_tr_b16 v[70:71], v4 offset:3072\n\t"
    "ds_read_b128 a[40:43], v5 offset:1024\n\t"
    "ds_read_b128 a[44:47], v14 offset:1024\n\t"
    "v_mfma_f32_32x32x16_bf16 v[16:31], v[76:79], a[56:59], v[16:31]\n\t"
    "v_mfma_f32_32x32x16_bf16 v[32:47], v[76:79], a[60:63], v[32:47]\n\t"
    "ds_read_b64_tr_b16 v[72:73], v4 offset:4096\n\t"
    "ds_read_b64_tr_b16 v[74:75], v4 offset:5120\n\t"
    "ds_read_b128 a[48:51], v5 offset:6144\n\t"
    "ds_read_b128 a[52:55], v14 offset:6144\n\t"
    "s_waitcnt lgkmcnt(15)\n\t"
    "v_mfma_f32_32x32x16_bf16 v[16:31], v[48:51], a[0:3], v[16:31]\n\t"
    "ds_read_b64_tr_b16 v[76:77], v4 offset:6144\n\t"
    "ds_read_b64_tr_b16 v[78:79], v4 offset:7168\n\t"
    "ds_read_b128 a[56:59], v5 offset:7168\n\t"
    "ds_read_b128 a[60:63], v14 offset:7168\n\t"
    "v_mfma_f32_32x32x16_bf16 v[16:31], v[52:55], a[8:11], v[16:31]\n\t"
    "v_mfma_f32_32x32x16_bf16 v[16:31], v[56:59], a[16:19], v[16:31]\n\t"
    "s_waitcnt lgkmcnt(15)\n\t"
    "v_mfma_f32_32x32x16_bf16 v[16:31], v[60:63], a[24:27], v[16:31]\n\t"
    "s_waitcnt lgkmcnt(12)\n\t"
    "v_mfma_f32_32x32x16_bf16 v[16:31], v[64:67], a[32:35], v[16:31]\n\t"
    "v_mfma_f32_32x32x16_bf16 v[32:47], v[64:67], a[36:39], v[32:47]\n\t"
    "s_waitcnt lgkmcnt(0)\n\t"
    "s_barrier\n\t"
    "v_add_u32 v12, 16384, v9\n\t"
    "v_add_u32 v13, 98304, v10\n\t"
    "v_add_u32 v14, 98304, v11\n\t"
    "v_add_u32 v4, 16384, v2\n\t"
    "v_add_u32 v5, 98304, v3\n\t"
    "ds_read_b64_tr_b16 v[48:49], v12 offset:0\n\t"
    "ds_read_b64_tr_b16 v[50:51], v12 offset:1024\n\t"
    "ds_read_b128 a[0:3], v13 offset:0\n\t"
    "ds_read_b64_tr_b16 v[52:53], v12 offset:2048\n\t"
    "ds_read_b64_tr_b16 v[54:55], v12 offset:3072\n\t"
    "ds_read_b128 a[8:11], v13 offset:1024\n\t"
    "ds_read_b64_tr_b16 v[56:57], v12 offset:4096\n\t"
    "ds_read_b64_tr_b16 v[58:59], v12 offset:5120\n\t"
    "ds_read_b128 a[16:19], v13 offset:6144\n\t"
    "ds_read_b64_tr_b16 v[60:61], v12 offset:6144\n\t"
    "ds_read_b64_tr_b16 v[62:63], v12 offset:7168\n\t"
    "ds_read_b128 a[24:27], v13 offset:7168\n\t"
    "v_mfma_f32_32x32x16_bf16 v[16:31], v[68:71], a[40:43], v[16:31]\n\t"
    "v_mfma_f32_32x32x16_bf16 v[32:47], v[68:71], a[44:47], v[32:47]\n\t"
    "ds_read_b64_tr_b16 v[64:65], v4 offset:0\n\t"
    "ds_read_b64_tr_b16 v[66:67], v4 offset:1024\n\t"
    "ds_read_b128 a[32:35], v5 offset:0\n\t"
    "ds_read_b128 a[36:39], v14 offset:0\n\t"
    "v_mfma_f32_32x32x16_bf16 v[16:31], v[72:75], a[48:51], v[16:31]\n\t"
    "v_mfma_f32_32x32x16_bf16 v[32:47], v[72:75], a[52:55], v[32:47]\n\t"
    "ds_read_b64_tr_b16 v[68:69], v4 offset:2048\n\t"
    "ds_read_b64_tr_b16 v[70:71], v4 offset:3072\n\t"
    "ds_read_b128 a[40:43], v5 offset:1024\n\t"
    "ds_read_b128 a[44:47], v14 offset:1024\n\t"
    "v_mfma_f32_32x32x16_bf16 v[16:31], v[76:79], a[56:59], v[16:31]\n\t"
    "v_mfma_f32_32x32x16_bf16 v[32:47], v[76:79], a[60:63], v[32:47]\n\t"
    "ds_read_b64_tr_b16 v[72:73], v4 offset:4096\n\t"
    "ds_read_b64_tr_b16 v[74:75], v4 offset:5120\n\t"
    "ds_read_b128 a[48:51], v5 offset:6144\n\t"
    "ds_read_b128 a[52:55], v14 offset:6144\n\t"
    "s_waitcnt lgkmcnt(15)\n\t"
    "v_mfma_f32_32x32x16_bf16 v[16:31], v[48:51], a[0:3], v[16:31]\n\t"
    "ds_read_b64_tr_b16 v[76:77], v4 offset:6144\n\t"
    "ds_read_b64_tr_b16 v[78:79], v4 offset:7168\n\t"
    "ds_read_b128 a[56:59], v5 offset:7168\n\t"
    "ds_read_b128 a[60:63], v14 offset:7168\n\t"
    "v_mfma_f32_32x32x16_bf16 v[16:31], v[52:55], a[8:11], v[16:31]\n\t"
    "v_mfma_f32_32x32x16_bf16 v[16:31], v[56:59], a[16:19], v[16:31]\n\t"
    "s_waitcnt lgkmcnt(15)\n\t"
    "v_mfma_f32_32x32x16_bf16 v[16:31], v[60:63], a[24:27], v[16:31]\n\t"
    "s_waitcnt lgkmcnt(12)\n\t"
    "v_mfma_f32_32x32x16_bf16 v[16:31], v[64:67], a[32:35], v[16:31]\n\t"
    "v_mfma_f32_32x32x16_bf16 v[32:47], v[64:67], a[36:39], v[32:47]\n\t"
    "s_waitcnt lgkmcnt(0)\n\t"
    "s_barrier\n\t"
    "v_add_u32 v12, 73728, v9\n\t"
    "v_add_u32 v13, 139264, v10\n\t"
    "v_add_u32 v14, 139264, v11\n\t"
    "v_add_u32 v4, 73728, v2\n\t"
    "v_add_u32 v5, 139264, v3\n\t"
    "ds_read_b64_tr_b16 v[48:49], v12 offset:0\n\t"
    "ds_read_b64_tr_b16 v[50:51], v12 offset:1024\n\t"
    "ds_read_b128 a[0:3], v13 offset:0\n\t"
    "ds_read_b64_tr_b16 v[52:53], v12 offset:2048\n\t"
    "ds_read_b64_tr_b16 v[54:55], v12 offset:3072\n\t"
    "ds_read_b128 a[8:11], v13 offset:1024\n\t"
    "ds_read_b64_tr_b16 v[56:57], v12 offset:4096\n\t"
    "ds_read_b64_tr_b16 v[58:59], v12 offset:5120\n\t"
    "ds_read_b128 a[16:19], v13 offset:6144\n\t"
    "ds_read_b64_tr_b16 v[60:61], v12 offset:6144\n\t"
    "ds_read_b64_tr_b16 v[62:63], v12 offset:7168\n\t"
    "ds_read_b128 a[24:27], v13 offset:7168\n\t"
    "v_mfma_f32_32x32x16_bf16 v[16:31], v[68:71], a[40:43], v[16:31]\n\t"
    "v_mfma_f32_32x32x16_bf16 v[32:47], v[68:71], a[44:47], v[32:47]\n\t"
    "ds_read_b64_tr_b16 v[64:65], v4 offset:0\n\t"
    "ds_read_b64_tr_b16 v[66:67], v4 offset:1024\n\t"
    "ds_read_b128 a[32:35], v5 offset:0\n\t"
    "ds_read_b128 a[36:39], v14 offset:0\n\t"
    "v_mfma_f32_32x32x16_bf16 v[16:31], v[72:75], a[48:51], v[16:31]\n\t"
    "v_mfma_f32_32x32x16_bf16 v[32:47], v[72:75], a[52:55], v[32:47]\n\t"
    "ds_read_b64_tr_b16 v[68:69], v4 offset:2048\n\t"
    "ds_read_b64_tr_b16 v[70:71], v4 offset:3072\n\t"
    "ds_read_b128 a[40:43], v5 offset:1024\n\t"
    "ds_read_b128 a[44:47], v14 offset:1024\n\t"
    "v_mfma_f32_32x32x16_bf16 v[16:31], v[76:79], a[56:59], v[16:31]\n\t"
    "v_mfma_f32_32x32x16_bf16 v[32:47], v[76:79], a[60:63], v[32:47]\n\t"
    "ds_read_b64_tr_b16 v[72:73], v4 offset:4096\n\t"
    "ds_read_b64_tr_b16 v[74:75], v4 offset:5120\n\t"
    "ds_read_b128 a[48:51], v5 offset:6144\n\t"
    "ds_read_b128 a[52:55], v14 offset:6144\n\t"
    "s_waitcnt lgkmcnt(15)\n\t"
    "v_mfma_f32_32x32x16_bf16 v[16:31], v[48:51], a[0:3], v[16:31]\n\t"
    "ds_read_b64_tr_b16 v[76:77], v4 offset:6144\n\t"
    "ds_read_b64_tr_b16 v[78:79], v4 offset:7168\n\t"
    "ds_read_b128 a[56:59], v5 offset:7168\n\t"
    "ds_read_b128 a[60:63], v14 offset:7168\n\t"
    "v_mfma_f32_32x32x16_bf16 v[16:31], v[52:55], a[8:11], v[16:31]\n\t"
    "v_mfma_f32_32x32x16_bf16 v[16:31], v[56:59], a[16:19], v[16:31]\n\t"
    "s_waitcnt lgkmcnt(15)\n\t"
    "v_mfma_f32_32x32x16_bf16 v[16:31], v[60:63], a[24:27], v[16:31]\n\t"
    "s_waitcnt lgkmcnt(12)\n\t"
    "v_mfma_f32_32x32x16_bf16 v[16:31], v[64:67], a[32:35], v[16:31]\n\t"
    "v_mfma_f32_32x32x16_bf16 v[32:47], v[64:67], a[36:39], v[32:47]\n\t"
    "s_waitcnt lgkmcnt(0)\n\t"
    "s_barrier\n\t"
    "s_cmp_lg_u32 s40, 0\n\t"
    "s_cbranch_scc1 L_np4_i_%=\n\t"
    "s_lshl_b32 s44, s28, 3\n\t"
    "s_add_u32 s44, s44, 4096\n\t"
    "v_lshl_add_u32 v82, v0, 9, s44\n\t"
    "buffer_load_dwordx2 v[94:95], v82, s[24:27], s57 offen sc1\n\t"
    "L_np4_i_%=:\n\t"
    "v_add_u32 v12, 0, v9\n\t"
    "v_add_u32 v13, 32768, v10\n\t"
    "v_add_u32 v14, 32768, v11\n\t"
    "v_add_u32 v4, 0, v2\n\t"
    "v_add_u32 v5, 32768, v3\n\t"
    "ds_read_b64_tr_b16 v[48:49], v12 offset:0\n\t"
    "ds_read_b64_tr_b16 v[50:51], v12 offset:1024\n\t"
    "ds_read_b128 a[0:3], v13 offset:0\n\t"
    "ds_read_b64_tr_b16 v[52:53], v12 offset:2048\n\t"
    "ds_read_b64_tr_b16 v[54:55], v12 offset:3072\n\t"
    "ds_read_b128 a[8:11], v13 offset:1024\n\t"
    "ds_read_b64_tr_b16 v[56:57], v12 offset:4096\n\t"
    "ds_read_b64_tr_b16 v[58:59], v12 offset:5120\n\t"
    "ds_read_b128 a[16:19], v13 offset:6144\n\t"
    "ds_read_b64_tr_b16 v[60:61], v12 offset:6144\n\t"
    "ds_read_b64_tr_b16 v[62:63], v12 offset:7168\n\t"
    "ds_read_b128 a[24:27], v13 offset:7168\n\t"
    "v_mfma_f32_32x32x16_bf16 v[16:31], v[68:71], a[40:43], v[16:31]\n\t"
    "v_mfma_f32_32x32x16_bf16 v[32:47], v[68:71], a[44:47], v[32:47]\n\t"
    "ds_read_b64_tr_b16 v[64:65], v4 offset:0\n\t"
    "ds_read_b64_tr_b16 v[66:67], v4 offset:1024\n\t"
    "ds_read_b128 a[32:35], v5 offset:0\n\t"
    "ds_read_b128 a[36:39], v14 offset:0\n\t"
    "v_mfma_f32_32x32x16_bf16 v[16:31], v[72:75], a[48:51], v[16:31]\n\t"
    "v_mfma_f32_32x32x16_bf16 v[32:47], v[72:75], a[52:55], v[32:47]\n\t"
    "ds_read_b64_tr_b16 v[68:69], v4 offset:2048\n\t"
    "ds_read_b64_tr_b16 v[70:71], v4 offset:3072\n\t"
    "ds_read_b128 a[40:43], v5 offset:1024\n\t"
    "ds_read_b128 a[44:47], v14 offset:1024\n\t"
    "v_mfma_f32_32x32x16_bf16 v[16:31], v[76:79], a[56:59], v[16:31]\n\t"
    "v_mfma_f32_32x32x16_bf16 v[32:47], v[76:79], a[60:63], v[32:47]\n\t"
    "ds_read_b64_tr_b16 v[72:73], v4 offset:4096\n\t"
    "ds_read_b64_tr_b16 v[74:75], v4 offset:5120\n\t"
    "ds_read_b128 a[48:51], v5 offset:6144\n\t"
    "ds_read_b128 a[52:55], v14 offset:6144\n\t"
    "s_waitcnt lgkmcnt(15)\n\t"
    "v_mfma_f32_32x32x16_bf16 v[16:31], v[48:51], a[0:3], v[16:31]\n\t"
    "ds_read_b64_tr_b16 v[76:77], v4 offset:6144\n\t"
    "ds_read_b64_tr_b16 v[78:79], v4 offset:7168\n\t"
    "ds_read_b128 a[56:59], v5 offset:7168\n\t"
    "ds_read_b128 a[60:63], v14 offset:7168\n\t"
    "v_mfma_f32_32x32x16_bf16 v[16:31], v[52:55], a[8:11], v[16:31]\n\t"
    "v_mfma_f32_32x32x16_bf16 v[16:31], v[56:59], a[16:19], v[16:31]\n\t"
    "s_waitcnt lgkmcnt(15)\n\t"
    "v_mfma_f32_32x32x16_bf16 v[16:31], v[60:63], a[24:27], v[16:31]\n\t"
    "s_waitcnt lgkmcnt(12)\n\t"
    "v_mfma_f32_32x32x16_bf16 v[16:31], v[64:67], a[32:35], v[16:31]\n\t"
    "v_mfma_f32_32x32x16_bf16 v[32:47], v[64:67], a[36:39], v[32:47]\n\t"
    "s_cmp_lg_u32 s40, 0\n\t"
    "s_cbranch_scc1 L_np4_done_%=\n\t"
    "s_mov_b32 s62, 0\n\t"
    "s_waitcnt vmcnt(0)\n\t"
    "L_np4_loop_%=:\n\t"
    "v_subrev_u32 v94, s60, v94\n\t"
    "v_cmp_eq_u32 vcc, s61, v95\n\t"
    "v_add_u32 v94, -1, v94\n\t"
    "v_cmp_ge_u32 s[82:83], 2, v94\n\t"
    "s_and_b64 vcc, vcc, s[82:83]\n\t"
    "v_cmp_le_u32 s[82:83], 2, v94\n\t"
    "s_and_b64 s[82:83], s[82:83], vcc\n\t"
    "s_andn2_b64 s[82:83], s[58:59], s[82:83]\n\t"
    "s_not_b64 vcc, vcc\n\t"
    "s_or_b64 s[82:83], s[82:83], vcc\n\t"
    "s_cmp_eq_u64 s[82:83], 0\n\t"
    "s_cbranch_scc1 L_np4_done_%=\n\t"
    "s_add_u32 s62, s62, 1\n\t"
    "s_cmp_gt_u32 s62, 0x4000\n\t"
    "s_cbranch_scc1 L_np4_done_%=\n\t"
    "s_and_b32 s44, s62, 3\n\t"
    "s_cmp_lg_u32 s44, 0\n\t"
    "s_cbranch_scc1 L_np4_same_%=\n\t"
    "v_lshlrev_b32 v82, 3, v0\n\t"
    "buffer_load_dwordx2 v[94:95], v82, s[24:27], s57 offen sc1\n\t"
    "s_waitcnt vmcnt(0)\n\t"
    "v_and_b32 v94, -4, v94\n\t"
    "v_cmp_ne_u64 vcc, v[94:95], v[92:93]\n\t"
    "s_cmp_eq_u64 vcc, 0\n\t"
    "s_cbranch_scc1 L_np4_same_%=\n\t"
    "v_mov_b32 v92, v94\n\t"
    "v_mov_b32 v93, v95\n\t"
    "v_add_u32 v86, 3, v92\n\t"
    "v_mov_b32 v87, v93\n\t"
    "s_lshl_b32 s44, s28, 9\n\t"
    "s_add_u32 s44, s44, 4096\n\t"
    "v_lshl_add_u32 v83, v0, 3, s44\n\t"
    "buffer_store_dwordx2 v[86:87], v83, s[24:27], s57 offen sc1\n\t"
    "L_np4_same_%=:\n\t"
    "s_lshl_b32 s44, s28, 3\n\t"
    "s_add_u32 s44, s44, 4096\n\t"
    "v_lshl_add_u32 v82, v0, 9, s44\n\t"
    "buffer_load_dwordx2 v[94:95], v82, s[24:27], s57 offen sc1\n\t"
    "s_waitcnt vmcnt(0)\n\t"
    "s_branch L_np4_loop_%=\n\t"
    "L_np4_done_%=:\n\t"
    "s_cmp_lg_u32 s40, 0\n\t"
    "s_cbranch_scc1 L_nfin_i_%=\n\t"
    "v_lshlrev_b32 v82, 3, v0\n\t"
    "buffer_load_dwordx2 v[94:95], v82, s[24:27], s57 offen sc1\n\t"
    "L_nfin_i_%=:\n\t"
    "s_waitcnt lgkmcnt(0)\n\t"
    "s_barrier\n\t"
    "v_add_u32 v12, 57344, v9\n\t"
    "v_add_u32 v13, 90112, v10\n\t"
    "v_add_u32 v14, 90112, v11\n\t"
    "v_add_u32 v4, 57344, v2\n\t"
    "v_add_u32 v5, 90112, v3\n\t"
    "ds_read_b64_tr_b16 v[48:49], v12 offset:0\n\t"
    "ds_read_b64_tr_b16 v[50:51], v12 offset:1024\n\t"
    "ds_read_b128 a[0:3], v13 offset:0\n\t"
    "ds_read_b64_tr_b16 v[52:53], v12 offset:2048\n\t"
    "ds_read_b64_tr_b16 v[54:55], v12 offset:3072\n\t"
    "ds_read_b128 a[8:11], v13 offset:1024\n\t"
    "ds_read_b64_tr_b16 v[56:57], v12 offset:4096\n\t"
    "ds_read_b64_tr_b16 v[58:59], v12 offset:5120\n\t"
    "ds_read_b128 a[16:19], v13 offset:6144\n\t"
    "ds_read_b64_tr_b16 v[60:61], v12 offset:6144\n\t"
    "ds_read_b64_tr_b16 v[62:63], v12 offset:7168\n\t"
    "ds_read_b128 a[24:27], v13 offset:7168\n\t"
    "v_mfma_f32_32x32x16_bf16 v[16:31], v[68:71], a[40:43], v[16:31]\n\t"
    "v_mfma_f32_32x32x16_bf16 v[32:47], v[68:71], a[44:47], v[32:47]\n\t"
    "ds_read_b64_tr_b16 v[64:65], v4 offset:0\n\t"
    "ds_read_b64_tr_b16 v[66:67], v4 offset:1024\n\t"
    "ds_read_b128 a[32:35], v5 offset:0\n\t"
    "ds_read_b128 a[36:39], v14 offset:0\n\t"
    "v_mfma_f32_32x32x16_bf16 v[16:31], v[72:75], a[48:51], v[16:31]\n\t"
    "v_mfma_f32_32x32x16_bf16 v[32:47], v[72:75], a[52:55], v[32:47]\n\t"
    "ds_read_b64_tr_b16 v[68:69], v4 offset:2048\n\t"
    "ds_read_b64_tr_b16 v[70:71], v4 offset:3072\n\t"
    "ds_read_b128 a[40:43], v5 offset:1024\n\t"
    "ds_read_b128 a[44:47], v14 offset:1024\n\t"
    "v_mfma_f32_32x32x16_bf16 v[16:31], v[76:79], a[56:59], v[16:31]\n\t"
    "v_mfma_f32_32x32x16_bf16 v[32:47], v[76:79], a[60:63], v[32:47]\n\t"
    "ds_read_b64_tr_b16 v[72:73], v4 offset:4096\n\t"
    "ds_read_b64_tr_b16 v[74:75], v4 offset:5120\n\t"
    "ds_read_b128 a[48:51], v5 offset:6144\n\t"
    "ds_read_b128 a[52:55], v14 offset:6144\n\t"
    "s_waitcnt lgkmcnt(15)\n\t"
    "v_mfma_f32_32x32x16_bf16 v[16:31], v[48:51], a[0:3], v[16:31]\n\t"
    "ds_read_b64_tr_b16 v[76:77], v4 offset:6144\n\t"
    "ds_read_b64_tr_b16 v[78:79], v4 offset:7168\n\t"
    "ds_read_b128 a[56:59], v5 offset:7168\n\t"
    "ds_read_b128 a[60:63], v14 offset:7168\n\t"
    "v_mfma_f32_32x32x16_bf16 v[16:31], v[52:55], a[8:11], v[16:31]\n\t"
    "v_mfma_f32_32x32x16_bf16 v[16:31], v[56:59], a[16:19], v[16:31]\n\t"
    "s_waitcnt lgkmcnt(15)\n\t"
    "v_mfma_f32_32x32x16_bf16 v[16:31], v[60:63], a[24:27], v[16:31]\n\t"
    "s_waitcnt lgkmcnt(12)\n\t"
    "v_mfma_f32_32x32x16_bf16 v[16:31], v[64:67], a[32:35], v[16:31]\n\t"
    "v_mfma_f32_32x32x16_bf16 v[32:47], v[64:67], a[36:39], v[32:47]\n\t"
    "s_waitcnt lgkmcnt(8)\n\t"
    "v_mfma_f32_32x32x16_bf16 v[16:31], v[68:71], a[40:43], v[16:31]\n\t"
    "v_mfma_f32_32x32x16_bf16 v[32:47], v[68:71], a[44:47], v[32:47]\n\t"
    "s_waitcnt lgkmcnt(4)\n\t"
    "v_mfma_f32_32x32x16_bf16 v[16:31], v[72:75], a[48:51], v[16:31]\n\t"
    "v_mfma_f32_32x32x16_bf16 v[32:47], v[72:75], a[52:55], v[32:47]\n\t"
    "s_waitcnt lgkmcnt(0)\n\t"
    "v_mfma_f32_32x32x16_bf16 v[16:31], v[76:79], a[56:59], v[16:31]\n\t"
    "v_mfma_f32_32x32x16_bf16 v[32:47], v[76:79], a[60:63], v[32:47]\n\t"
    "s_barrier\n\t"
    "s_lshr_b32 s43, s30, 1\n\t"
    "s_mul_i32 s44, s43, 4608\n\t"
    "v_add_u32 v7, s44, v15\n\t"
    "s_mul_i32 s44, s43, 8704\n\t"
    "s_and_b32 s45, s30, 1\n\t"
    "s_mul_i32 s45, s45, 4352\n\t"
    "s_add_u32 s44, s44, s45\n\t"
    "s_add_u32 s44, s44, 16384\n\t"
    "v_add_u32 v8, s44, v1\n\t"
    "s_mul_i32 s44, s30, 1152\n\t"
    "v_add_u32 v13, s44, v80\n\t"
    "v_lshrrev_b32 v4, 3, v0\n\t"
    "s_lshl_b32 s44, s30, 3\n\t"
    "v_add_u32 v4, s44, v4\n\t"
    "v_and_b32 v5, 7, v0\n\t"
    "s_movk_i32 s44, 1088\n\t"
    "v_mul_lo_u32 v5, v5, s44\n\t"
    "v_lshl_add_u32 v4, v4, 4, v5\n\t"
    "v_add_u32 v4, 16384, v4\n\t"
    "s_mul_i32 s44, s30, 10240\n\t"
    "s_add_u32 s51, s51, s44\n\t"
    "v_cvt_pk_bf16_f32 v48, v16, v17\n\t"
    "v_cvt_pk_bf16_f32 v49, v18, v19\n\t"
    "ds_write_b64 v7, v[48:49] offset:0\n\t"
    "v_cvt_pk_bf16_f32 v50, v20, v21\n\t"
    "v_cvt_pk_bf16_f32 v51, v22, v23\n\t"
    "ds_write_b64 v7, v[50:51] offset:16\n\t"
    "v_cvt_pk_bf16_f32 v52, v24, v25\n\t"
    "v_cvt_pk_bf16_f32 v53, v26, v27\n\t"
    "ds_write_b64 v7, v[52:53] offset:32\n\t"
    "v_cvt_pk_bf16_f32 v54, v28, v29\n\t"
    "v_cvt_pk_bf16_f32 v55, v30, v31\n\t"
    "ds_write_b64 v7, v[54:55] offset:48\n\t"
    "s_cmp_eq_u32 s56, 2\n\t"
    "s_cbranch_scc1 L_nob_%=\n\t"
    "ds_write_b128 v8, v[32:35] offset:0\n\t"
    "ds_write_b128 v8, v[36:39] offset:1088\n\t"
    "ds_write_b128 v8, v[40:43] offset:2176\n\t"
    "ds_write_b128 v8, v[44:47] offset:3264\n\t"
    "L_nob_%=:\n\t"
    "s_waitcnt lgkmcnt(0)\n\t"
    "s_barrier\n\t"
    "ds_read_b128 v[64:67], v13\n\t"
    "ds_read_b128 v[68:71], v13 offset:4608\n\t"
    "ds_read_b128 v[16:19], v4 offset:0\n\t"
    "ds_read_b128 v[20:23], v4 offset:512\n\t"
    "ds_read_b128 v[24:27], v4 offset:8704\n\t"
    "ds_read_b128 v[28:31], v4 offset:9216\n\t"
    "s_add_u32 s54, s51, 40960\n\t"
    "s_add_u32 s55, s51, 81920\n\t"
    "s_waitcnt lgkmcnt(5)\n\t"
    "buffer_store_dwordx4 v[64:67], v81, s[20:23], s51 offen sc1\n\t"
    "s_waitcnt lgkmcnt(4)\n\t"
    "buffer_store_dwordx4 v[68:71], v81, s[20:23], s54 offen sc1\n\t"
    "s_cmp_eq_u32 s56, 2\n\t"
    "s_cbranch_scc1 L_o2_%=\n\t"
    "s_waitcnt lgkmcnt(0)\n\t"
    "v_add_f32 v16, v16, v24\n\t"
    "v_add_f32 v17, v17, v25\n\t"
    "v_add_f32 v18, v18, v26\n\t"
    "v_add_f32 v19, v19, v27\n\t"
    "v_add_f32 v20, v20, v28\n\t"
    "v_add_f32 v21, v21, v29\n\t"
    "v_add_f32 v22, v22, v30\n\t"
    "v_add_f32 v23, v23, v31\n\t"
    "v_cvt_pk_bf16_f32 v72, v16, v17\n\t"
    "v_cvt_pk_bf16_f32 v73, v18, v19\n\t"
    "v_cvt_pk_bf16_f32 v74, v20, v21\n\t"
    "v_cvt_pk_bf16_f32 v75, v22, v23\n\t"
    "buffer_store_dwordx4 v[72:75], v81, s[20:23], s55 offen sc1\n\t"
    "L_o2_%=:\n\t"
    "s_cmp_lg_u32 s40, 0\n\t"
    "s_cbranch_scc1 L_nfin_done_%=\n\t"
    "s_waitcnt vmcnt(0)\n\t"
    "v_and_b32 v94, -4, v94\n\t"
    "v_cmp_ne_u64 vcc, v[94:95], v[92:93]\n\t"
    "s_cmp_eq_u64 vcc, 0\n\t"
    "s_cbranch_scc1 L_nfin_done_%=\n\t"
    "v_mov_b32 v92, v94\n\t"
    "v_mov_b32 v93, v95\n\t"
    "v_add_u32 v86, 3, v92\n\t"
    "v_mov_b32 v87, v93\n\t"
    "s_lshl_b32 s44, s28, 9\n\t"
    "s_add_u32 s44, s44, 4096\n\t"
    "v_lshl_add_u32 v83, v0, 3, s44\n\t"
    "buffer_store_dwordx2 v[86:87], v83, s[24:27], s57 offen sc1\n\t"
    "L_nfin_done_%=:\n\t"
    "s_branch L_end_%=\n\t"
    "L_cspare_%=:\n\t"
    "s_cmp_lg_u32 s40, 0\n\t"
    "s_cbranch_scc1 L_spe2_%=\n\t"
    "v_add_u32 v86, 2, v92\n\t"
    "v_mov_b32 v87, v93\n\t"
    "s_lshl_b32 s44, s28, 9\n\t"
    "s_add_u32 s44, s44, 4096\n\t"
    "v_lshl_add_u32 v83, v0, 3, s44\n\t"
    "buffer_store_dwordx2 v[86:87], v83, s[24:27], s57 offen sc1\n\t"
    "L_spe2_%=:\n\t"
    "v_maximum3_f32 v16, v16, 0, 0\n\t"
    "v_maximum3_f32 v17, v17, 0, 0\n\t"
    "v_maximum3_f32 v18, v18, 0, 0\n\t"
    "v_maximum3_f32 v19, v19, 0, 0\n\t"
    "v_maximum3_f32 v20, v20, 0, 0\n\t"
    "v_maximum3_f32 v21, v21, 0, 0\n\t"
    "v_maximum3_f32 v22, v22, 0, 0\n\t"
    "v_maximum3_f32 v23, v23, 0, 0\n\t"
    "v_maximum3_f32 v24, v24, 0, 0\n\t"
    "v_maximum3_f32 v25, v25, 0, 0\n\t"
    "v_maximum3_f32 v26, v26, 0, 0\n\t"
    "v_maximum3_f32 v27, v27, 0, 0\n\t"
    "v_maximum3_f32 v28, v28, 0, 0\n\t"
    "v_maximum3_f32 v29, v29, 0, 0\n\t"
    "v_maximum3_f32 v30, v30, 0, 0\n\t"
    "v_maximum3_f32 v31, v31, 0, 0\n\t"
    "v_cvt_pk_bf16_f32 v84, v16, v17\n\t"
    "v_cvt_pk_bf16_f32 v85, v18, v19\n\t"
    "v_cvt_pk_bf16_f32 v86, v20, v21\n\t"
    "v_cvt_pk_bf16_f32 v87, v22, v23\n\t"
    "v_cvt_pk_bf16_f32 v88, v24, v25\n\t"
    "v_cvt_pk_bf16_f32 v89, v26, v27\n\t"
    "v_cvt_pk_bf16_f32 v90, v28, v29\n\t"
    "v_cvt_pk_bf16_f32 v91, v30, v31\n\t"
    "s_barrier\n\t"
    "s_lshl_b32 s55, s99, 5\n\t"
    "s_add_u32 s55, s55, s49\n\t"
    "buffer_store_dwordx4 v[84:87], v1, s[16:19], s55 offen sc1\n\t"
    "buffer_store_dwordx4 v[88:91], v1, s[16:19], s55 offen offset:1024 sc1\n\t"
    "s_barrier\n\t"
    "s_barrier\n\t"
    "s_waitcnt vmcnt(0)\n\t"
    "s_barrier\n\t"
    "s_cmp_lg_u32 s40, 0\n\t"
    "s_cbranch_scc1 L_spe3_%=\n\t"
    "v_add_u32 v86, 3, v92\n\t"
    "v_mov_b32 v87, v93\n\t"
    "s_lshl_b32 s44, s28, 9\n\t"
    "s_add_u32 s44, s44, 4096\n\t"
    "v_lshl_add_u32 v83, v0, 3, s44\n\t"
    "buffer_store_dwordx2 v[86:87], v83, s[24:27], s57 offen sc1\n\t"
    "L_spe3_%=:\n\t"
    "s_barrier\n\t"
    "s_cmp_lg_u32 s40, 0\n\t"
    "s_cbranch_scc1 L_spp3_i_%=\n\t"
    "s_lshl_b32 s44, s28, 3\n\t"
    "s_add_u32 s44, s44, 4096\n\t"
    "v_lshl_add_u32 v82, v0, 9, s44\n\t"
    "buffer_load_dwordx2 v[94:95], v82, s[24:27], s57 offen sc1\n\t"
    "L_spp3_i_%=:\n\t"
    "s_cmp_lg_u32 s40, 0\n\t"
    "s_cbranch_scc1 L_spp3_done_%=\n\t"
    "s_mov_b32 s62, 0\n\t"
    "s_waitcnt vmcnt(0)\n\t"
    "L_spp3_loop_%=:\n\t"
    "v_subrev_u32 v94, s60, v94\n\t"
    "v_cmp_eq_u32 vcc, s61, v95\n\t"
    "v_add_u32 v94, -1, v94\n\t"
    "v_cmp_ge_u32 s[82:83], 2, v94\n\t"
    "s_and_b64 vcc, vcc, s[82:83]\n\t"
    "v_cmp_le_u32 s[82:83], 2, v94\n\t"
    "s_and_b64 s[82:83], s[82:83], vcc\n\t"
    "s_andn2_b64 s[82:83], s[58:59], s[82:83]\n\t"
    "s_not_b64 vcc, vcc\n\t"
    "s_or_b64 s[82:83], s[82:83], vcc\n\t"
    "s_cmp_eq_u64 s[82:83], 0\n\t"
    "s_cbranch_scc1 L_spp3_done_%=\n\t"
    "s_add_u32 s62, s62, 1\n\t"
    "s_cmp_gt_u32 s62, 0x4000\n\t"
    "s_cbranch_scc1 L_spp3_done_%=\n\t"
    "s_and_b32 s44, s62, 3\n\t"
    "s_cmp_lg_u32 s44, 0\n\t"
    "s_cbranch_scc1 L_spp3_same_%=\n\t"
    "v_lshlrev_b32 v82, 3, v0\n\t"
    "buffer_load_dwordx2 v[94:95], v82, s[24:27], s57 offen sc1\n\t"
    "s_waitcnt vmcnt(0)\n\t"
    "v_and_b32 v94, -4, v94\n\t"
    "v_cmp_ne_u64 vcc, v[94:95], v[92:93]\n\t"
    "s_cmp_eq_u64 vcc, 0\n\t"
    "s_cbranch_scc1 L_spp3_same_%=\n\t"
    "v_mov_b32 v92, v94\n\t"
    "v_mov_b32 v93, v95\n\t"
    "v_add_u32 v86, 3, v92\n\t"
    "v_mov_b32 v87, v93\n\t"
    "s_lshl_b32 s44, s28, 9\n\t"
    "s_add_u32 s44, s44, 4096\n\t"
    "v_lshl_add_u32 v83, v0, 3, s44\n\t"
    "buffer_store_dwordx2 v[86:87], v83, s[24:27], s57 offen sc1\n\t"
    "L_spp3_same_%=:\n\t"
    "s_lshl_b32 s44, s28, 3\n\t"
    "s_add_u32 s44, s44, 4096\n\t"
    "v_lshl_add_u32 v82, v0, 9, s44\n\t"
    "buffer_load_dwordx2 v[94:95], v82, s[24:27], s57 offen sc1\n\t"
    "s_waitcnt vmcnt(0)\n\t"
    "s_branch L_spp3_loop_%=\n\t"
    "L_spp3_done_%=:\n\t"
    "s_cmp_lg_u32 s40, 0\n\t"
    "s_cbranch_scc1 L_spfin_i_%=\n\t"
    "v_lshlrev_b32 v82, 3, v0\n\t"
    "buffer_load_dwordx2 v[94:95], v82, s[24:27], s57 offen sc1\n\t"
    "L_spfin_i_%=:\n\t"
    "s_cmp_lg_u32 s40, 0\n\t"
    "s_cbranch_scc1 L_spfin_done_%=\n\t"
    "s_waitcnt vmcnt(0)\n\t"
    "v_and_b32 v94, -4, v94\n\t"
    "v_cmp_ne_u64 vcc, v[94:95], v[92:93]\n\t"
    "s_cmp_eq_u64 vcc, 0\n\t"
    "s_cbranch_scc1 L_spfin_done_%=\n\t"
    "v_mov_b32 v92, v94\n\t"
    "v_mov_b32 v93, v95\n\t"
    "v_add_u32 v86, 3, v92\n\t"
    "v_mov_b32 v87, v93\n\t"
    "s_lshl_b32 s44, s28, 9\n\t"
    "s_add_u32 s44, s44, 4096\n\t"
    "v_lshl_add_u32 v83, v0, 3, s44\n\t"
    "buffer_store_dwordx2 v[86:87], v83, s[24:27], s57 offen sc1\n\t"
    "L_spfin_done_%=:\n\t"
    "L_end_%=:\n\t"
    "L_fin_%=:\n\t"
    :
    : "s"(__builtin_amdgcn_kernarg_segment_ptr()), "s"(blockIdx.x), "v"(threadIdx.x), "s"((unsigned)(uintptr_t)(__attribute__((address_space(3))) char*)lds)
    : "v1", "v2", "v3", "v4", "v5", "v6", "v7", "v8", "v9", "v10", "v11", "v12", "v13", "v14", "v15", "v16", "v17", "v18", "v19", "v20", "v21", "v22", "v23", "v24", "v25", "v26", "v27", "v28", "v29", "v30", "v31", "v32", "v33", "v34", "v35", "v36", "v37", "v38", "v39", "v40", "v41", "v42", "v43", "v44", "v45", "v46", "v47", "v48", "v49", "v50", "v51", "v52", "v53", "v54", "v55", "v56", "v57", "v58", "v59", "v60", "v61", "v62", "v63", "v64", "v65", "v66", "v67", "v68", "v69", "v70", "v71", "v72", "v73", "v74", "v75", "v76", "v77", "v78", "v79", "v80", "v81", "v82", "v83", "v84", "v85", "v86", "v87", "v88", "v89", "v90", "v91", "v92", "v93", "v94", "v95", "a0", "a1", "a2", "a3", "a4", "a5", "a6", "a7", "a8", "a9", "a10", "a11", "a12", "a13", "a14", "a15", "a16", "a17", "a18", "a19", "a20", "a21", "a22", "a23", "a24", "a25", "a26", "a27", "a28", "a29", "a30", "a31", "a32", "a33", "a34", "a35", "a36", "a37", "a38", "a39", "a40", "a41", "a42", "a43", "a44", "a45", "a46", "a47", "a48", "a49", "a50", "a51", "a52", "a53", "a54", "a55", "a56", "a57", "a58", "a59", "a60", "a61", "a62", "a63", "a64", "a65", "a66", "a67", "a68", "a69", "a70", "a71", "a72", "a73", "a74", "a75", "a76", "a77", "a78", "a79", "a80", "a81", "a82", "a83", "a84", "a85", "a86", "a87", "a88", "a89", "a90", "a91", "a92", "a93", "a94", "a95", "a96", "a97", "a98", "a99", "a100", "a101", "a102", "a103", "a104", "a105", "a106", "a107", "a108", "a109", "a110", "a111", "a112", "a113", "a114", "a115", "a116", "a117", "a118", "a119", "a120", "a121", "a122", "a123", "a124", "a125", "a126", "a127", "a128", "a129", "a130", "a131", "a132", "a133", "a134", "a135", "a136", "a137", "a138", "a139", "a140", "a141", "a142", "a143", "a144", "a145", "a146", "a147", "a148", "a149", "a150", "a151", "a152", "a153", "a154", "a155", "a156", "a157", "a158", "a159", "s4", "s5", "s6", "s7", "s8", "s9", "s10", "s11", "s12", "s13", "s14", "s15", "s16", "s17", "s18", "s19", "s20", "s21", "s22", "s23", "s24", "s25", "s26", "s27", "s28", "s29", "s30", "s31", "s34", "s35", "s36", "s37", "s38", "s39", "s40", "s41", "s42", "s43", "s44", "s45", "s46", "s47", "s48", "s49", "s50", "s51", "s52", "s53", "s54", "s55", "s56", "s57", "s58", "s59", "s60", "s61", "s62", "s63", "s64", "s65", "s66", "s67", "s68", "s69", "s70", "s71", "s72", "s73", "s74", "s75", "s76", "s77", "s78", "s79", "s80", "s81", "s82", "s83", "s84", "s85", "s86", "s87", "s88", "s89", "s90", "s91", "s92", "s93", "s94", "s95", "s96", "s97", "s98", "s99", "vcc", "scc", "memory");
}

extern "C" void kernel_launch(void* const* d_in, const int* in_sizes, int n_in, void* d_out, int out_size, void* d_ws, size_t ws_size, hipStream_t stream) {
  (void)in_sizes; (void)n_in; (void)out_size; (void)ws_size;
  standalone__dense_activation_relu__prompt_kernel<<<256, 512, 0, stream>>>(d_in[0], d_in[1], d_in[2], d_out, d_ws);
}
```
